```python
import torch

def _softmax(x, axis=-1):
    return torch.softmax(x, dim=axis)
def _gelu(x):
    return torch.nn.functional.gelu(x, approximate="tanh")
def _silu(x):
    return torch.nn.functional.silu(x)
def _sigmoid(x):
    return torch.sigmoid(x)
def _layernorm(x, axis=-1, eps=1e-5):
    m = x.mean(dim=axis, keepdim=True)
    v = x.var(dim=axis, keepdim=True, unbiased=False)
    return (x - m) / torch.sqrt(v + eps)
def _scatter_add(tgt, idx, src, axis=0):
    return tgt.clone().index_add_(axis, idx, src)
def _segment_sum(src, seg, n):
    out = torch.zeros((n,) + src.shape[1:], dtype=src.dtype)
    return out.index_add_(0, seg, src)
def _pad(x, pw):
    flat = tuple(v for pair in reversed(pw) for v in pair)
    return torch.nn.functional.pad(x, flat)

kernel_name = 'interleave_layers__ssm_conv_s4d__dense_activation_mish__prompt'

def setup_inputs():

    _w = (lambda a: (lambda t: t.to(torch.bfloat16) if t.is_floating_point() else t)(torch.as_tensor(a)))
    def _A_setup():
        Bsz, L, D, N = 1, 2048, 512, 128
        return {
            "u": _w((lambda *s: torch.randn(*s, dtype=torch.bfloat16) * 0.05)(Bsz, L, D)),
            "log_A_real": _w((lambda *s: torch.randn(*s, dtype=torch.bfloat16) * 3.2)(D, N) * 0.5),
            "A_imag": _w((lambda *s: torch.randn(*s, dtype=torch.bfloat16) * 0.05)(D, N) * 3.14),
            "C_real": _w((lambda *s: torch.randn(*s, dtype=torch.bfloat16) * 0.05)(D, N) / N ** 0.5),
            "C_imag": _w((lambda *s: torch.randn(*s, dtype=torch.bfloat16) * 0.05)(D, N) / N ** 0.5),
            "log_dt": _w((lambda *s: torch.randn(*s, dtype=torch.bfloat16) * 0.05)(D) * 0.5 - 4.0),
            "D_skip": _w((lambda *s: torch.randn(*s, dtype=torch.bfloat16) * 0.05)(D)),
        }
    _A_in = _A_setup()

    def _B_setup():
        B, S, D, F = 1, 2048, 512, 1792
        return {
            "x": _w((lambda *s: torch.randn(*s, dtype=torch.bfloat16) * 0.05)(B, S, D)),
            "w1": _w((lambda *s: torch.randn(*s, dtype=torch.bfloat16) * 0.8)(D, F) * (D ** -0.5)),
            "b1": _w((lambda *s: torch.randn(*s, dtype=torch.bfloat16) * 0.2)(F)),
            "w2": _w((lambda *s: torch.randn(*s, dtype=torch.bfloat16) * 0.2)(F, D) * (F ** -0.5)),
        }
    _B_in = _B_setup()

    return {
        "A_u": _A_in["u"],
        "A_log_A_real": _A_in["log_A_real"],
        "A_A_imag": _A_in["A_imag"],
        "A_C_real": _A_in["C_real"],
        "A_C_imag": _A_in["C_imag"],
        "A_log_dt": _A_in["log_dt"],
        "A_D_skip": _A_in["D_skip"],
        "B_x": _B_in["x"],
        "B_w1": _B_in["w1"],
        "B_b1": _B_in["b1"],
        "B_w2": _B_in["w2"],
    }

def reference(A_u, A_log_A_real, A_A_imag, A_C_real, A_C_imag, A_log_dt, A_D_skip, B_x, B_w1, B_b1, B_w2):
    def _A_ref(u, log_A_real, A_imag, C_real, C_imag, log_dt, D_skip):
        L = u.shape[1]
        dt = torch.exp(log_dt)
        A = -torch.exp(log_A_real) + 1j * A_imag
        C = (C_real + 1j * C_imag) * dt[:, None]
        pos = (lambda x, dt: x.to(dt))(torch.arange(L), log_dt.dtype)
        P = torch.exp((A * dt[:, None])[:, :, None] * pos[None, None, :])
        K = 2.0 * torch.einsum("dn,dnl->dl", C, P).real
        L2 = 2 * L
        u_t = (lambda x, *d: x.permute(*d))(u, 0, 2, 1)
        Kf = (lambda x, *a, **k: torch.fft.rfft(x.float(), *a, **k))(K, L2)
        Uf = (lambda x, *a, **k: torch.fft.rfft(x.float(), *a, **k))(u_t, L2)
        y = (lambda x, *a, **k: torch.fft.irfft(x, *a, **k).to(torch.bfloat16))(Uf * Kf[None, :, :], L2)[:, :, :L]
        y = (lambda x, *d: x.permute(*d))(y, 0, 2, 1)
        return y + u * D_skip[None, None, :]

    def _B_ref(x, w1, b1, w2):
        h = torch.einsum("bsd,df->bsf", x, w1) + b1
        sp = torch.log1p(torch.exp(-torch.abs(h))) + torch.where(h > 0, h, torch.zeros_like(h))
        act = h * torch.tanh(sp)
        return torch.einsum("bsf,fd->bsd", act, w2)

    x = A_u
    x = x + _A_ref(_layernorm(x, axis=-1), A_log_A_real, A_A_imag, A_C_real, A_C_imag, A_log_dt, A_D_skip)
    x = x + _B_ref(_layernorm(x, axis=-1), B_w1, B_b1, B_w2)
    x = x + _A_ref(_layernorm(x, axis=-1), A_log_A_real, A_A_imag, A_C_real, A_C_imag, A_log_dt, A_D_skip)
    x = x + _B_ref(_layernorm(x, axis=-1), B_w1, B_b1, B_w2)
    return x
```

```cpp
#include <hip/hip_runtime.h>
#include <hip/hip_bf16.h>
#include <stdint.h>
#define EXACT_POS 1
typedef unsigned short u16;
constexpr int L = 2048, D = 512, N = 128, F = 1792;
__device__ __forceinline__ float bf2f(u16 b) { return __uint_as_float(((unsigned)b) << 16); }
__device__ __forceinline__ u16 f2bf(float f) { unsigned u = __float_as_uint(f); u += 0x7fffu + ((u >> 16) & 1u); return (u16)(u >> 16); }
__device__ __forceinline__ float rbf(float f) { return bf2f(f2bf(f)); }

__global__ void kgen(const u16* lar, const u16* aim, const u16* cr, const u16* ci, const u16* ldt, float* K) {
  int d = blockIdx.y; int l = blockIdx.x * 256 + threadIdx.x;
  float dt = rbf(expf(bf2f(ldt[d])));
  float pos = EXACT_POS ? (float)l : rbf((float)l);
  float s = 0.f;
  for (int n = 0; n < N; ++n) {
    float ar = -rbf(expf(bf2f(lar[d * N + n]))) * dt, ai = bf2f(aim[d * N + n]) * dt;
    float cre = bf2f(cr[d * N + n]) * dt, cim = bf2f(ci[d * N + n]) * dt;
    float e = expf(ar * pos); float sn, cs; sincosf(ai * pos, &sn, &cs);
    s += e * (cre * cs - cim * sn);
  }
  K[d * L + l] = 2.f * s;
}
__global__ void ln(const u16* x, u16* u) {
  int l = blockIdx.x; int t = threadIdx.x;
  __shared__ float red[256];
  float a = bf2f(x[l * D + t]), b = bf2f(x[l * D + t + 256]);
  red[t] = a + b; __syncthreads();
  for (int s = 128; s > 0; s >>= 1) { if (t < s) red[t] += red[t + s]; __syncthreads(); }
  float m = red[0] / D; __syncthreads();
  red[t] = (a - m) * (a - m) + (b - m) * (b - m); __syncthreads();
  for (int s = 128; s > 0; s >>= 1) { if (t < s) red[t] += red[t + s]; __syncthreads(); }
  float v = red[0] / D; float r = rsqrtf(v + 1e-5f);
  u[l * D + t] = f2bf((a - m) * r); u[l * D + t + 256] = f2bf((b - m) * r);
}
__global__ void conv(const u16* x, const u16* u, const float* K, const u16* dsk, u16* xo) {
  int l = blockIdx.x; int d = blockIdx.y * 256 + threadIdx.x;
  float s = 0.f;
  for (int j = 0; j <= l; ++j) s += K[d * L + (l - j)] * bf2f(u[j * D + d]);
  float y = rbf(s); float t = rbf(bf2f(u[l * D + d]) * bf2f(dsk[d])); float a = rbf(y + t);
  xo[l * D + d] = f2bf(bf2f(x[l * D + d]) + a);
}
__global__ void g1(const u16* u, const u16* w1, const u16* b1, u16* H) {
  int l = blockIdx.x; int f = blockIdx.y * 256 + threadIdx.x;
  float s = 0.f;
  for (int k = 0; k < D; ++k) s += bf2f(u[l * D + k]) * bf2f(w1[k * F + f]);
  float h = s + bf2f(b1[f]);
  float t = expf(-fabsf(h)); float th;
  if (h >= 0.f) th = (1.f + 2.f * t) / (1.f + 2.f * t + 2.f * t * t); else { float n = t * (t + 2.f); th = n / (n + 2.f); }
  H[l * F + f] = f2bf(h * th);
}
__global__ void g2(const u16* H, const u16* w2, const u16* x, u16* xo) {
  int l = blockIdx.x; int d = blockIdx.y * 256 + threadIdx.x;
  float s = 0.f;
  for (int k = 0; k < F; ++k) s += bf2f(H[l * F + k]) * bf2f(w2[k * D + d]);
  xo[l * D + d] = f2bf(bf2f(x[l * D + d]) + rbf(s));
}
extern "C" void kernel_launch(void* const* d_in, const int* in_sizes, int n_in, void* d_out, int out_size, void* d_ws, size_t ws_size, hipStream_t stream) {
  const u16* x0 = (const u16*)d_in[0];
  char* ws = (char*)d_ws;
  float* K = (float*)ws; u16* u = (u16*)(ws + (4 << 20)); u16* x1 = (u16*)(ws + (8 << 20)); u16* x2 = (u16*)(ws + (12 << 20)); u16* x3 = (u16*)(ws + (16 << 20)); u16* H = (u16*)(ws + (20 << 20));
  kgen<<<dim3(L / 256, D), 256, 0, stream>>>((const u16*)d_in[1], (const u16*)d_in[2], (const u16*)d_in[3], (const u16*)d_in[4], (const u16*)d_in[5], K);
  const u16* dsk = (const u16*)d_in[6]; const u16* w1 = (const u16*)d_in[8]; const u16* b1 = (const u16*)d_in[9]; const u16* w2 = (const u16*)d_in[10];
  ln<<<L, 256, 0, stream>>>(x0, u); conv<<<dim3(L, 2), 256, 0, stream>>>(x0, u, K, dsk, x1);
  ln<<<L, 256, 0, stream>>>(x1, u); g1<<<dim3(L, 7), 256, 0, stream>>>(u, w1, b1, H); g2<<<dim3(L, 2), 256, 0, stream>>>(H, w2, x1, x2);
  ln<<<L, 256, 0, stream>>>(x2, u); conv<<<dim3(L, 2), 256, 0, stream>>>(x2, u, K, dsk, x3);
  ln<<<L, 256, 0, stream>>>(x3, u); g1<<<dim3(L, 7), 256, 0, stream>>>(u, w1, b1, H); g2<<<dim3(L, 2), 256, 0, stream>>>(H, w2, x3, (u16*)d_out);
}
```

```cpp
#include <hip/hip_runtime.h>
#include <stdint.h>

typedef unsigned short u16;
typedef unsigned long long u64;
typedef unsigned v4u __attribute__((ext_vector_type(4)));
typedef unsigned v2u __attribute__((ext_vector_type(2)));
typedef float f32x2 __attribute__((ext_vector_type(2)));
typedef float f32x4 __attribute__((ext_vector_type(4)));
typedef float f32x16 __attribute__((ext_vector_type(16)));
typedef __bf16 bf16x8 __attribute__((ext_vector_type(8)));
typedef __bf16 bf16x2 __attribute__((ext_vector_type(2)));
typedef short s16x4 __attribute__((ext_vector_type(4)));
#define LAS __attribute__((address_space(3)))
#define GAS __attribute__((address_space(1)))
#define DI __device__ __forceinline__

constexpr int L_ = 2048, D_ = 512, N_ = 128, F_ = 1792;
constexpr int WS_EPOCH = 0;
constexpr int WS_ST0 = 4096;
constexpr int WS_ST2 = 32768;
constexpr int WS_W1F = 65536;
constexpr int WS_W2F = WS_W1F + 1792 * 1024;
constexpr int WS_XT1 = WS_W2F + 1792 * 1024;
constexpr int WS_XT2 = WS_XT1 + (2 << 20);
constexpr int WS_XT3 = WS_XT2 + (2 << 20);
constexpr int WS_H1 = WS_XT3 + (2 << 20);
constexpr int H_BYTES = 32 * 2 * 112 * 1024;
constexpr int WS_H2 = WS_H1 + H_BYTES;
constexpr int WS_END = WS_H2 + H_BYTES;
constexpr int WS_DBG = 32 << 20;
constexpr int KCS = 4160;
constexpr int LD_K = 0;
constexpr int LD_XT = 33280;
constexpr int LD_F = LD_XT + 65536;
constexpr int LD_U = LD_F + 4096;
constexpr int LD_X = LD_U + 2 * 4608;
constexpr int LD_EX = LD_X + 2 * 4096;
constexpr int LD_ST = LD_EX + 16384;
constexpr int LD_P1 = LD_ST + 2 * 4736;
constexpr int LD_P2 = LD_P1 + 2048;
constexpr int LD_CB = LD_P2 + 4096;
constexpr int LD_FL = LD_CB + 2048;
constexpr int LD_END = LD_FL + 64;
static_assert(LD_END <= 163840, "lds");
constexpr int TROW = 528;
constexpr unsigned POISON = 0xFFFFFFFFu;
constexpr int SPIN_CAP = 1 << 14;
constexpr int SC1 = 16;

DI float bf2f(unsigned b) { return __uint_as_float(b << 16); }
DI float bflo(unsigned p) { return __uint_as_float(p << 16); }
DI float bfhi(unsigned p) { return __uint_as_float(p & 0xffff0000u); }
DI unsigned pk(float a, float b) { f32x2 f = {a, b}; return __builtin_bit_cast(unsigned, __builtin_convertvector(f, bf16x2)); }
DI float rbf(float a) { return bflo(pk(a, a)); }
DI unsigned umax3(unsigned a, unsigned b, unsigned c) { return max(max(a, b), c); }
DI unsigned pz(unsigned m, v4u v) { return umax3(umax3(m, v.x, v.y), v.z, v.w); }
DI f32x16 mfma32(v4u a, v4u b, f32x16 c) { return __builtin_amdgcn_mfma_f32_32x32x16_bf16(__builtin_bit_cast(bf16x8, a), __builtin_bit_cast(bf16x8, b), c, 0, 0, 0); }
DI f32x4 mfma16(v4u a, v4u b, f32x4 c) { return __builtin_amdgcn_mfma_f32_16x16x32_bf16(__builtin_bit_cast(bf16x8, a), __builtin_bit_cast(bf16x8, b), c, 0, 0, 0); }
DI v2u trread(LAS unsigned char* p) { return __builtin_bit_cast(v2u, __builtin_amdgcn_ds_read_tr16_b64_v4i16((LAS s16x4*)p)); }
DI float dot2(unsigned a, unsigned b, float c) { return __builtin_amdgcn_fdot2_f32_bf16(__builtin_bit_cast(bf16x2, a), __builtin_bit_cast(bf16x2, b), c, false); }
#define CFENCE() asm volatile("" ::: "memory")
#define BAR() do { asm volatile("s_waitcnt lgkmcnt(0)" ::: "memory"); __builtin_amdgcn_s_barrier(); asm volatile("" ::: "memory"); } while (0)
#define DRAIN() asm volatile("s_waitcnt vmcnt(0)" ::: "memory")
template <int CTRL> DI float dppadd(float x) { return x + __uint_as_float(__builtin_amdgcn_update_dpp(0u, __float_as_uint(x), CTRL, 0xf, 0xf, false)); }
DI float swap32add(float a, float b) { auto r = __builtin_amdgcn_permlane32_swap(__float_as_uint(a), __float_as_uint(b), false, false); return __uint_as_float(r[0]) + __uint_as_float(r[1]); }
DI float swap16add(float a, float b) { auto r = __builtin_amdgcn_permlane16_swap(__float_as_uint(a), __float_as_uint(b), false, false); return __uint_as_float(r[0]) + __uint_as_float(r[1]); }
struct cpx { float re, im; };
DI cpx cmul(cpx a, cpx b) { return {a.re * b.re - a.im * b.im, a.re * b.im + a.im * b.re}; }
DI cpx cexp_(float ar2, float rev, float p) {
  float e = __builtin_amdgcn_exp2f(ar2 * p);
  float t = __builtin_amdgcn_fractf(rev * p);
  return {e * __builtin_amdgcn_cosf(t), e * __builtin_amdgcn_sinf(t)};
}
DI void ln_consts(float sum, float sq, float& m, float& s) {
  float mean = sum * (1.0f / D_);
  float var = fmaxf(sq * (1.0f / D_) - mean * mean, 0.f);
  m = rbf(mean);
  s = rbf(__builtin_amdgcn_sqrtf(rbf(rbf(var) + 1e-5f)));
}

template <int GRP> DI void conv_loop(LAS unsigned char* fp, LAS unsigned char* gp, f32x4 (&acc)[4]) {
  constexpr int C0 = GRP ? 1 : 0, C1 = GRP ? 2 : 3, C2 = GRP ? 5 : 4, C3 = GRP ? 6 : 7;
#pragma unroll 1
  for (int r4 = 0; r4 < 4; ++r4) {
    v4u Fr[C3 + 1], Gr[C3 + 1];
#pragma unroll
    for (int p = 0; p <= C3; ++p) {
      const v2u lo = *(volatile LAS v2u*)(fp + 512 * (7 - p)), hi = *(volatile LAS v2u*)(fp + 512 * (7 - p) + 8);
      Fr[p] = v4u{lo.x, lo.y, hi.x, hi.y};
      Gr[p] = *(LAS v4u*)(gp + 512 * p);
    }
#pragma unroll
    for (int p = 0; p <= C0; ++p) acc[0] = mfma16(Fr[p], Gr[C0 - p], acc[0]);
#pragma unroll
    for (int p = 0; p <= C1; ++p) acc[1] = mfma16(Fr[p], Gr[C1 - p], acc[1]);
#pragma unroll
    for (int p = 0; p <= C2; ++p) acc[2] = mfma16(Fr[p], Gr[C2 - p], acc[2]);
#pragma unroll
    for (int p = 0; p <= C3; ++p) acc[3] = mfma16(Fr[p], Gr[C3 - p], acc[3]);
    fp -= 64; gp -= 64;
  }
}

#define STAMP(i)

__global__ void __launch_bounds__(512, 2)
fused_fwd(const u16* __restrict__ x0, const u16* __restrict__ lar, const u16* __restrict__ aim, const u16* __restrict__ crp,
          const u16* __restrict__ cip, const u16* __restrict__ ldt, const u16* __restrict__ dsk, const u16* __restrict__ w1,
          const u16* __restrict__ b1, const u16* __restrict__ w2, u16* __restrict__ out, unsigned char* ws) {
  extern __shared__ __attribute__((aligned(16))) unsigned char smem_[];
  LAS unsigned char* sm = (LAS unsigned char*)smem_;
  int tid = threadIdx.x, lane = tid & 63;
  const int w = __builtin_amdgcn_readfirstlane(tid >> 6);
#define OPQ() do { asm volatile("" : "+v"(tid)); lane = tid & 63; } while (0)
  const int bid = blockIdx.x, xcd = bid & 7, slot = bid >> 3;
  const int rp = xcd * 4 + (slot >> 3), cp = slot & 7, pj = xcd * 32 + slot;
  const __amdgpu_buffer_rsrc_t rs = __builtin_amdgcn_make_buffer_rsrc((void*)ws, 0, WS_END, 0x00020000);
  const v4u PZ4 = {POISON, POISON, POISON, POISON};
  STAMP(0);

  const int kch = w >> 2, role = ((w >> 1) ^ (w >> 2)) & 1, mode_n = (w & 1) * 64 + lane, dk = 2 * pj + kch;
  const unsigned r_lar = lar[dk * N_ + mode_n], r_aim = aim[dk * N_ + mode_n], r_cr = crp[dk * N_ + mode_n], r_ci = cip[dk * N_ + mode_n];
  const unsigned r_ldt = ldt[dk];
  const v4u xr = *(const v4u*)(x0 + (bid * 8 + w) * D_ + lane * 8);
  const unsigned epoch = __builtin_amdgcn_raw_buffer_load_b32(rs, WS_EPOCH, 0, SC1);
  unsigned g1[8], g2[8];
  int w1dst = 0, w2dst = 0;
  if (w < 7) {
    const int h = lane >> 5, r = lane & 31;
    const int ks = bid >> 3, cw = bid & 7;
    const u16* s1 = w1 + (16 * ks + 8 * h) * F_ + 224 * cw + 32 * w + r;
    const int idx = bid * 7 + w, ksg = idx >> 4, dtile = idx & 15;
    const u16* s2 = w2 + (16 * ksg + 4 * h) * D_ + 32 * dtile + r;
#pragma unroll
    for (int j = 0; j < 8; ++j) g1[j] = s1[j * F_];
#pragma unroll
    for (int j = 0; j < 8; ++j) g2[j] = s2[(8 * (j >> 2) + (j & 3)) * D_];
    w1dst = WS_W1F + ((cw * 7 + w) * 32 + ks) * 1024 + lane * 16;
    const int c2 = ksg / 14, rem = ksg - 14 * c2;
    const int sl = rem < 8 ? 16 * (rem >> 1) + 2 * c2 + (rem & 1) : 64 + 6 * c2 + (rem - 8);
    w2dst = WS_W2F + (((dtile >> 1) * 112 + sl) * 2 + (dtile & 1)) * 1024 + lane * 16;
  }
  unsigned xg[4];
#pragma unroll
  for (int i = 0; i < 4; ++i) xg[i] = *(const unsigned*)(x0 + (tid * 4 + i) * D_ + 2 * pj);
  const unsigned r_dsk = *(const unsigned*)(dsk + 2 * pj);
  const unsigned r_b1 = b1[224 * cp + min(tid, 223)];
  __builtin_amdgcn_raw_buffer_store_b128(PZ4, rs, WS_XT1 + bid * 8192 + tid * 16, 0, SC1);
  if (tid < 64) *(LAS v4u*)(sm + LD_U + (tid >> 5) * 4608 + (tid & 31) * 16) = v4u{0, 0, 0, 0};
  if (tid < 16) *(LAS unsigned*)(sm + LD_FL + tid * 4) = 0u;

  unsigned gran;
  {
    float s = 0.f, q = 0.f;
#pragma unroll
    for (int i = 0; i < 4; ++i) { s = dot2(xr[i], 0x3f803f80u, s); q = dot2(xr[i], xr[i], q); }
    float t = swap32add(s, q);
    t = dppadd<0xB1>(t); t = dppadd<0x4E>(t); t = dppadd<0x141>(t); t = dppadd<0x140>(t);
    t = swap16add(t, t);
    const float sum = __uint_as_float(__builtin_amdgcn_readlane(__float_as_uint(t), 0));
    const float sq = __uint_as_float(__builtin_amdgcn_readlane(__float_as_uint(t), 32));
    float m, sd; ln_consts(sum, sq, m, sd);
    gran = (__float_as_uint(m) >> 16) | (__float_as_uint(sd) & 0xffff0000u);
  }
  if (w < 7) {
    v4u a, b;
#pragma unroll
    for (int j = 0; j < 4; ++j) { a[j] = g1[2 * j] | (g1[2 * j + 1] << 16); b[j] = g2[2 * j] | (g2[2 * j + 1] << 16); }
    __builtin_amdgcn_raw_buffer_store_b128(a, rs, w1dst, 0, SC1);
    __builtin_amdgcn_raw_buffer_store_b128(b, rs, w2dst, 0, SC1);
  }
  if (tid < 224) *(LAS float*)(sm + LD_CB + 1024 + tid * 4) = bf2f(r_b1);
  const unsigned tag = epoch * 0x9E3779B1u + 0x7F4A7C15u;
  STAMP(1);
  {
    const float dt = rbf(__expf(bf2f(r_ldt)));
    const float ar = -rbf(__expf(bf2f(r_lar))) * dt, ai = bf2f(r_aim) * dt;
    const float ar2 = ar * 1.44269504088896f, rev = ai * 0.159154943091895f;
    LAS unsigned char* tb = sm + LD_XT + (kch * 2 + role) * (32 * TROW) + mode_n * 4;
    cpx e1 = cexp_(ar2, rev, role ? 8.f : 1.f);
    cpx e2 = role ? cpx{2.f * bf2f(r_cr) * dt, 2.f * bf2f(r_ci) * dt} : cexp_(ar2, rev, 256.f);
    DRAIN();
    if (lane == 0) { v2u gv = {gran, tag}; __builtin_amdgcn_raw_buffer_store_b64(gv, rs, WS_ST0 + (bid * 8 + w) * 8, 0, SC1); }
    STAMP(2);
    {
      const int hb = WS_H1 + bid * 28672 + tid * 16;
      __builtin_amdgcn_raw_buffer_store_b128(PZ4, rs, hb, 0, SC1);
      __builtin_amdgcn_raw_buffer_store_b128(PZ4, rs, hb + 8192, 0, SC1);
      __builtin_amdgcn_raw_buffer_store_b128(PZ4, rs, hb + 16384, 0, SC1);
      if (tid < 256) __builtin_amdgcn_raw_buffer_store_b128(PZ4, rs, hb + 24576, 0, SC1);
      __builtin_amdgcn_raw_buffer_store_b128(PZ4, rs, WS_XT2 + bid * 8192 + tid * 16, 0, SC1);
      if (tid < 8) __builtin_amdgcn_raw_buffer_store_b128(v4u{0, 0, 0, 0}, rs, WS_ST2 + bid * 128 + tid * 16, 0, SC1);
    }
#define PUTA(row, v) *(LAS unsigned*)(tb + (row) * TROW) = pk((v).re, -(v).im)
#define PUTB(row, v) *(LAS unsigned*)(tb + (row) * TROW) = pk((v).re, (v).im)
    if (role == 0) {
      const cpx p2 = cmul(e1, e1), p3 = cmul(p2, e1), p4 = cmul(p2, p2), p5 = cmul(p4, e1), p6 = cmul(p4, p2), p7 = cmul(p4, p3);
      const cpx W2 = cmul(e2, e2), W3 = cmul(W2, e2), W4 = cmul(W2, W2);
      *(LAS unsigned*)(tb) = 0x00003f80u;
      PUTA(1, e1); PUTA(2, p2); PUTA(3, p3); PUTA(4, p4); PUTA(5, p5); PUTA(6, p6); PUTA(7, p7);
      PUTA(8, e2); PUTA(9, cmul(e2, p2)); PUTA(10, cmul(e2, p4)); PUTA(11, cmul(e2, p6));
      PUTA(12, W2); PUTA(13, cmul(W2, p4)); PUTA(14, W3); PUTA(15, cmul(W3, p4));
      PUTA(16, W4); PUTA(17, cmul(W4, e2)); PUTA(18, cmul(W4, W2)); PUTA(19, cmul(W4, W3)); PUTA(20, cmul(W4, W4));
    } else {
      const cpx q2 = cmul(e1, e1), q4 = cmul(q2, q2);
      cpx b0 = e2, b1_ = cmul(e2, e1), b2 = cmul(e2, q2), b3 = cmul(b1_, q2);
#pragma unroll
      for (int a = 0; a < 8; ++a) {
        PUTB(4 * a, b0); PUTB(4 * a + 1, b1_); PUTB(4 * a + 2, b2); PUTB(4 * a + 3, b3);
        if (a < 7) { b0 = cmul(b0, q4); b1_ = cmul(b1_, q4); b2 = cmul(b2, q4); b3 = cmul(b3, q4); }
      }
    }
  }
  BAR();
  STAMP(3);
  {
    const int rt = (w >> 1) & 1, ct = w & 1, i16 = lane & 15, g = lane >> 4;
    LAS unsigned char* ta = sm + LD_XT + (kch * 2) * (32 * TROW) + (16 * rt + i16) * TROW + g * 16;
    LAS unsigned char* tbp = sm + LD_XT + (kch * 2 + 1) * (32 * TROW) + (16 * ct + i16) * TROW + g * 16;
    f32x4 acc = {0.f, 0.f, 0.f, 0.f};
#pragma unroll
    for (int ks = 0; ks < 8; ++ks) acc = mfma16(*(LAS v4u*)(ta + ks * 64), *(LAS v4u*)(tbp + ks * 64), acc);
    const int j8 = 8 * (16 * ct + i16);
#pragma unroll
    for (int reg = 0; reg < 4; ++reg) {
      int e;
      if (rt == 0) e = g == 0 ? reg : g == 1 ? 4 + reg : g == 2 ? 256 + 2 * reg : 512 + 256 * (reg >> 1) + 4 * (reg & 1);
      else e = g == 0 ? 1024 + 256 * reg : (g == 1 && reg == 0) ? 2048 : 2049;
      *(LAS u16*)(sm + LD_ST + kch * 4736 + 2 * (e + j8)) = (u16)pk(acc[reg], acc[reg]);
    }
  }
  BAR();
  {
#pragma unroll 1
    for (int it = 0; it < 2; ++it) {
      const int ch = it == 0 ? (tid >> 8) : (tid >> 1) & 1;
      const int y0 = it == 0 ? 16 + 8 * (tid & 255) : 2064 + 8 * (tid & 1);
      if (it == 1 && tid >= 4) break;
      unsigned kv[11];
#pragma unroll
      for (int m = 0; m < 11; ++m) {
        const int l = 2067 - y0 - m;
        const int lc = min(max(l, 0), 2048);
        const int p2 = (int)rbf((float)(2 * lc));
        const unsigned v = *(LAS u16*)(sm + LD_ST + ch * 4736 + p2);
        kv[m] = l < 0 ? 0u : v;
      }
#pragma unroll
      for (int s = 0; s < 4; ++s) {
        v4u o;
#pragma unroll
        for (int e = 0; e < 4; ++e) o[e] = kv[3 - s + 2 * e] | (kv[4 - s + 2 * e] << 16);
        *(LAS v4u*)(sm + LD_K + (ch * 4 + s) * KCS + 2 * y0) = o;
      }
    }
  }
  STAMP(4);

  float xa[4], xb[4], mu[4], rsd[4];
  {
      for (int sp = 0; sp < SPIN_CAP; ++sp) {
        CFENCE();
        const v4u a = __builtin_amdgcn_raw_buffer_load_b128(rs, WS_ST0 + tid * 32, 0, SC1);
        const v4u b = __builtin_amdgcn_raw_buffer_load_b128(rs, WS_ST0 + tid * 32 + 16, 0, SC1);
        mu[0] = bflo(a.x); mu[1] = bflo(a.z); mu[2] = bflo(b.x); mu[3] = bflo(b.z);
        rsd[0] = bfhi(a.x); rsd[1] = bfhi(a.z); rsd[2] = bfhi(b.x); rsd[3] = bfhi(b.z);
        const bool ok = a.y == tag && a.w == tag && b.y == tag && b.w == tag;
        if (__all(ok)) break;
      }
#pragma unroll
      for (int i = 0; i < 4; ++i) { xa[i] = bflo(xg[i]); xb[i] = bfhi(xg[i]); }
    DRAIN();
  }
  v4u W[32];
#pragma unroll 1
  for (int layer = 0; layer < 2; ++layer) {
    STAMP(5 + 12 * layer);
    OPQ();
    {
      float ua[4], ub[4];
#pragma unroll
      for (int i = 0; i < 4; ++i) { const float r = __builtin_amdgcn_rcpf(rsd[i]); ua[i] = rbf(xa[i] - mu[i]) * r; ub[i] = rbf(xb[i] - mu[i]) * r; }
      *(LAS v2u*)(sm + LD_U + 512 + tid * 8) = v2u{pk(ua[0], ua[1]), pk(ua[2], ua[3])};
      *(LAS v2u*)(sm + LD_U + 4608 + 512 + tid * 8) = v2u{pk(ub[0], ub[1]), pk(ub[2], ub[3])};
      *(LAS v2u*)(sm + LD_X + tid * 8) = v2u{pk(xa[0], xa[1]), pk(xa[2], xa[3])};
      *(LAS v2u*)(sm + LD_X + 4096 + tid * 8) = v2u{pk(xb[0], xb[1]), pk(xb[2], xb[3])};
    }
    BAR();
    if (layer == 0 && bid == 0 && tid == 0) __builtin_amdgcn_raw_buffer_store_b32(tag, rs, WS_EPOCH, 0, SC1);
    {
      const int wb = w < 7 ? WS_W1F + (cp * 7 + w) * 32768 : WS_W2F + (cp * 112 + 100) * 2048;
#pragma unroll
      for (int i = 0; i < 24; ++i) W[i] = __builtin_amdgcn_raw_buffer_load_b128(rs, lane * 16 + (i & 3) * 1024, wb + (i >> 2) * 4096, SC1);
      if (w < 7) {
#pragma unroll
        for (int i = 24; i < 32; ++i) W[i] = __builtin_amdgcn_raw_buffer_load_b128(rs, lane * 16 + (i & 3) * 1024, wb + (i >> 2) * 4096, SC1);
      }
    }
    __builtin_amdgcn_sched_barrier(0);
    STAMP(6 + 12 * layer);
    OPQ();
    {
      const int ch = w >> 2, hr = (w >> 1) & 1, grp = w & 1, b = lane & 15, g = lane >> 4;
      f32x4 acc[4];
#pragma unroll
      for (int i = 0; i < 4; ++i) acc[i] = f32x4{0.f, 0.f, 0.f, 0.f};
      LAS unsigned char* fp = sm + LD_K + (ch * 4 + (b & 3)) * KCS + 4096 + 16 * g - 2 * (b & ~3) - 3584 - 256 * hr;
      LAS unsigned char* gp = sm + LD_U + ch * 4608 + 480 + 32 * b + 16 * g - 256 * hr;
      if (grp == 0) conv_loop<0>(fp, gp, acc); else conv_loop<1>(fp, gp, acc);
      STAMP(7 + 12 * layer);
      f32x4 k0 = hr ? acc[2] : acc[0], k1 = hr ? acc[3] : acc[1];
      const f32x4 s0 = hr ? acc[0] : acc[2], s1 = hr ? acc[1] : acc[3];
      *(LAS f32x4*)(sm + LD_EX + w * 2048 + lane * 16) = s0;
      *(LAS f32x4*)(sm + LD_EX + w * 2048 + 1024 + lane * 16) = s1;
      const int cA = grp ? (hr ? 5 : 1) : (hr ? 4 : 0), cB = grp ? (hr ? 6 : 2) : (hr ? 7 : 3);
      const int po = 32 * b + 8 * g;
      const v2u uA = *(LAS v2u*)(sm + LD_U + ch * 4608 + 512 + 512 * cA + po), uB = *(LAS v2u*)(sm + LD_U + ch * 4608 + 512 + 512 * cB + po);
      const v2u xA = *(LAS v2u*)(sm + LD_X + ch * 4096 + 512 * cA + po), xB = *(LAS v2u*)(sm + LD_X + ch * 4096 + 512 * cB + po);
      const float ds = ch ? bfhi(r_dsk) : bflo(r_dsk);
      k0 += f32x4{bflo(xA.x) + bflo(uA.x) * ds, bfhi(xA.x) + bfhi(uA.x) * ds, bflo(xA.y) + bflo(uA.y) * ds, bfhi(xA.y) + bfhi(uA.y) * ds};
      k1 += f32x4{bflo(xB.x) + bflo(uB.x) * ds, bfhi(xB.x) + bfhi(uB.x) * ds, bflo(xB.y) + bflo(uB.y) * ds, bfhi(xB.y) + bfhi(uB.y) * ds};
      BAR();
      k0 += *(LAS f32x4*)(sm + LD_EX + (w ^ 2) * 2048 + lane * 16);
      k1 += *(LAS f32x4*)(sm + LD_EX + (w ^ 2) * 2048 + 1024 + lane * 16);
      const auto t0 = __builtin_amdgcn_permlane16_swap(pk(k0[0], k0[1]), pk(k1[0], k1[1]), false, false);
      const auto t1 = __builtin_amdgcn_permlane16_swap(pk(k0[2], k0[3]), pk(k1[2], k1[3]), false, false);
      const v4u o = {t0[0], t1[0], t0[1], t1[1]};
      const int cS = (g & 1) ? cB : cA;
      __builtin_amdgcn_raw_buffer_store_b128(o, rs, (layer ? WS_XT3 : WS_XT1) + (2 * pj + ch) * 4096 + 512 * cS + 32 * b + 16 * (g >> 1), 0, SC1);
    }
    STAMP(8 + 12 * layer);
    OPQ();
    if (w < 7 && layer == 0) {
      f32x16 cs;
#pragma unroll
      for (int i = 0; i < 16; ++i) cs[i] = 0.f;
      const v4u ones = {0x3f803f80u, 0x3f803f80u, 0x3f803f80u, 0x3f803f80u};
#pragma unroll
      for (int ks = 0; ks < 32; ++ks) cs = mfma32(W[ks], ones, cs);
      if ((lane & 31) == 0) {
#pragma unroll
        for (int g = 0; g < 4; ++g) *(LAS f32x4*)(sm + LD_CB + w * 128 + g * 32 + (lane >> 5) * 16) = f32x4{cs[4 * g], cs[4 * g + 1], cs[4 * g + 2], cs[4 * g + 3]};
      }
    }
    {
      v4u xv[8];
      const int xo = (layer ? WS_XT3 : WS_XT1) + (tid >> 3) * 4096 + rp * 128 + (tid & 7) * 16;
      for (int sp = 0; sp < SPIN_CAP; ++sp) {
        CFENCE();
#pragma unroll
        for (int i = 0; i < 8; ++i) xv[i] = __builtin_amdgcn_raw_buffer_load_b128(rs, xo, i * 262144, SC1);
        unsigned m = 0;
#pragma unroll
        for (int i = 0; i < 8; ++i) m = pz(m, xv[i]);
        if (!__any(m == POISON)) break;
      }
      STAMP(9 + 12 * layer);
      LAS unsigned char* xd = sm + LD_XT + (tid >> 3) * 128 + (((tid & 7) ^ (((tid >> 4) & 1) << 2)) * 16);
#pragma unroll
      for (int i = 0; i < 8; ++i) *(LAS v4u*)(xd + i * 8192) = xv[i];
    }
    BAR();
    STAMP(10 + 12 * layer);
    OPQ();
    const int q4 = (lane & 15) >> 2;
    LAS unsigned char* xt0 = sm + LD_XT + (8 * (lane >> 5) + q4) * 128 + ((lane >> 4) & 1) * 32 + 8 * (lane & 3);
    if (w >= 4) {
#pragma unroll
      for (int lt = 0; lt < 2; ++lt) {
        LAS unsigned char* xp = xt0 + ((lt ^ (q4 >> 1)) * 64) + (w - 4) * 16384;
        float s = 0.f, q = 0.f;
#pragma unroll
        for (int ks = 0; ks < 8; ++ks) {
          const v2u lo = trread(xp + ks * 2048), hi = trread(xp + ks * 2048 + 512);
          s = dot2(lo.x, 0x3f803f80u, s); q = dot2(lo.x, lo.x, q); s = dot2(lo.y, 0x3f803f80u, s); q = dot2(lo.y, lo.y, q);
          s = dot2(hi.x, 0x3f803f80u, s); q = dot2(hi.x, hi.x, q); s = dot2(hi.y, 0x3f803f80u, s); q = dot2(hi.y, hi.y, q);
        }
        *(LAS float*)(sm + LD_P1 + lt * 1024 + lane * 16 + (w - 4) * 4) = swap32add(s, q);
      }
      BAR();
      if (w == 7 && layer == 0) {
#pragma unroll 1
      for (int i = 0; i < 28; ++i) __builtin_amdgcn_raw_buffer_store_b128(PZ4, rs, WS_H2 + bid * 28672 + i * 1024 + lane * 16, 0, SC1);
#pragma unroll 1
      for (int i = 0; i < 8; ++i) __builtin_amdgcn_raw_buffer_store_b128(PZ4, rs, WS_XT3 + bid * 8192 + i * 1024 + lane * 16, 0, SC1);
      }
    }
    const int hbase = (layer ? WS_H2 : WS_H1) + rp * (2 * 112 * 1024);
    if (w < 7) {
      const int hslot = w < 4 ? 16 * w + 2 * cp : 64 + 6 * cp + 2 * (w - 4);
#pragma unroll 1
      for (int lt = 0; lt < 2; ++lt) {
        LAS unsigned char* xp = xt0 + ((lt ^ (q4 >> 1)) * 64);
        f32x16 acc;
#pragma unroll
        for (int i = 0; i < 16; ++i) acc[i] = 0.f;
#pragma unroll
        for (int ks = 0; ks < 32; ++ks) {
          const v2u lo = trread(xp + ks * 2048), hi = trread(xp + ks * 2048 + 512);
          acc = mfma32(W[ks], v4u{lo.x, lo.y, hi.x, hi.y}, acc);
        }
        if (w < 4 && lt == 0) BAR();
        const f32x4 ps = *(LAS f32x4*)(sm + LD_P1 + lt * 1024 + (lane & 31) * 16), pq = *(LAS f32x4*)(sm + LD_P1 + lt * 1024 + 512 + (lane & 31) * 16);
        float m, sd; ln_consts((ps[0] + ps[1]) + (ps[2] + ps[3]), (pq[0] + pq[1]) + (pq[2] + pq[3]), m, sd);
        const float r = __builtin_amdgcn_rcpf(sd);
        unsigned hp[8];
#pragma unroll
        for (int g = 0; g < 4; ++g) {
          const f32x4 c4 = *(LAS f32x4*)(sm + LD_CB + w * 128 + g * 32 + (lane >> 5) * 16), b4 = *(LAS f32x4*)(sm + LD_CB + 1024 + w * 128 + g * 32 + (lane >> 5) * 16);
          float o4[4];
#pragma unroll
          for (int e = 0; e < 4; ++e) {
            const float h = (acc[4 * g + e] - m * c4[e]) * r + b4[e];
            const float ex = __builtin_amdgcn_exp2f(fminf(h * 1.44269504088896f, 30.f));
            const float n = ex * (ex + 2.f);
            o4[e] = h * n * __builtin_amdgcn_rcpf(n + 2.f);
          }
          hp[2 * g] = pk(o4[0], o4[1]); hp[2 * g + 1] = pk(o4[2], o4[3]);
        }
        const int ho = hbase + (lt * 112 + hslot) * 1024 + lane * 16;
        __builtin_amdgcn_raw_buffer_store_b128(v4u{hp[0], hp[1], hp[2], hp[3]}, rs, ho, 0, SC1);
        __builtin_amdgcn_raw_buffer_store_b128(v4u{hp[4], hp[5], hp[6], hp[7]}, rs, ho + 1024, 0, SC1);
      }
      const int wb = WS_W2F + (cp * 112 + (w < 4 ? 16 * w : 64 + 12 * (w - 4))) * 2048;
#pragma unroll
      for (int i = 0; i < 24; ++i) W[i] = __builtin_amdgcn_raw_buffer_load_b128(rs, lane * 16 + (i & 3) * 1024, wb + (i >> 2) * 4096, SC1);
      if (w < 4) {
#pragma unroll
        for (int i = 24; i < 32; ++i) W[i] = __builtin_amdgcn_raw_buffer_load_b128(rs, lane * 16 + (i & 3) * 1024, wb + (i >> 2) * 4096, SC1);
      }
    }
    STAMP(11 + 12 * layer);
    OPQ();
    const v4u xres = *(LAS v4u*)(sm + LD_XT + (64 * cp + 8 * w + (lane >> 3)) * 128 + (((lane & 7) ^ (((lane >> 4) & 1) << 2)) * 16));
    {
      const int hs = hbase + (w < 4 ? 16 * w : 64 + 12 * (w - 4)) * 1024;
      v4u Hb[16];
#pragma unroll
      for (int i = 0; i < 12; ++i) Hb[i] = __builtin_amdgcn_raw_buffer_load_b128(rs, lane * 16 + (i & 3) * 1024, hs + (i >> 2) * 4096, SC1);
      if (w < 4) {
#pragma unroll
        for (int i = 12; i < 16; ++i) Hb[i] = __builtin_amdgcn_raw_buffer_load_b128(rs, lane * 16 + (i & 3) * 1024, hs + (i >> 2) * 4096, SC1);
      } else BAR();
      LAS unsigned char* slab = sm + LD_XT + (w & 3) * 16384;
      volatile LAS unsigned* fl = (volatile LAS unsigned*)(sm + LD_FL + (w & 3) * 8);
#pragma unroll
      for (int lt = 0; lt < 2; ++lt) {
        f32x16 a0, a1;
        if (w < 4) {
#pragma unroll
          for (int i = 0; i < 16; ++i) { a0[i] = 0.f; a1[i] = 0.f; }
        } else {
          for (int sp = 0; sp < (SPIN_CAP << 6); ++sp) { if (fl[lt] == (unsigned)(layer + 1)) break; __builtin_amdgcn_s_sleep(1); }
          CFENCE();
#pragma unroll
          for (int g = 0; g < 4; ++g) {
            const int so = (lt * 8 + g) * 1024 + ((lane ^ (2 * (lt * 4 + g))) << 4);
            const f32x4 t0 = *(LAS f32x4*)(slab + so), t1 = *(LAS f32x4*)(slab + so + 4096);
#pragma unroll
            for (int e = 0; e < 4; ++e) { a0[4 * g + e] = t0[e]; a1[4 * g + e] = t1[e]; }
          }
        }
#pragma unroll
        for (int gq = 0; gq < 4; ++gq) {
          if (gq < 3 || w < 4) {
            for (int sp = 0; sp < SPIN_CAP; ++sp) {
              const unsigned m = pz(pz(pz(pz(0u, Hb[4 * gq]), Hb[4 * gq + 1]), Hb[4 * gq + 2]), Hb[4 * gq + 3]);
              if (!__any(m == POISON)) break;
              CFENCE();
#pragma unroll
              for (int i = 0; i < 4; ++i) Hb[4 * gq + i] = __builtin_amdgcn_raw_buffer_load_b128(rs, lane * 16 + i * 1024, hs + lt * (112 * 1024) + gq * 4096, SC1);
            }
#pragma unroll
            for (int i = 0; i < 4; ++i) {
              a0 = mfma32(Hb[4 * gq + i], W[2 * (4 * gq + i)], a0);
              a1 = mfma32(Hb[4 * gq + i], W[2 * (4 * gq + i) + 1], a1);
            }
            if (lt == 0) {
#pragma unroll
              for (int i = 0; i < 4; ++i) Hb[4 * gq + i] = __builtin_amdgcn_raw_buffer_load_b128(rs, lane * 16 + i * 1024, hs + 112 * 1024 + gq * 4096, SC1);
            }
          }
        }
        if (w < 4 && lt == 0) BAR();
#pragma unroll
        for (int g = 0; g < 4; ++g) {
          const int so = (lt * 8 + g) * 1024 + ((lane ^ (2 * (lt * 4 + g))) << 4);
          *(LAS f32x4*)(slab + so) = f32x4{a0[4 * g], a0[4 * g + 1], a0[4 * g + 2], a0[4 * g + 3]};
          *(LAS f32x4*)(slab + so + 4096) = f32x4{a1[4 * g], a1[4 * g + 1], a1[4 * g + 2], a1[4 * g + 3]};
        }
        if (w < 4) { asm volatile("s_waitcnt lgkmcnt(0)" ::: "memory"); if (lane == 0) fl[lt] = (unsigned)(layer + 1); }
      }
    }
    STAMP(12 + 12 * layer);
    if (w == 7) DRAIN();
    BAR();
    STAMP(13 + 12 * layer);
    OPQ();
    {
      const int blk = lane & 7, r = (w & 3) * 8 + (lane >> 3);
      LAS unsigned char* sp = sm + LD_XT + ((blk >> 2) * 8 + (w >> 2) * 4 + (blk & 3)) * 1024 + ((r ^ (2 * blk)) << 4);
      f32x4 lo = {bflo(xres.x), bfhi(xres.x), bflo(xres.y), bfhi(xres.y)}, hi = {bflo(xres.z), bfhi(xres.z), bflo(xres.w), bfhi(xres.w)};
      f32x4 sl = {0.f, 0.f, 0.f, 0.f}, sh = {0.f, 0.f, 0.f, 0.f};
#pragma unroll
      for (int v = 0; v < 4; ++v) { sl += *(LAS f32x4*)(sp + v * 16384); sh += *(LAS f32x4*)(sp + v * 16384 + 512); }
      lo += sl; hi += sh;
      const v4u o = {pk(lo[0], lo[1]), pk(lo[2], lo[3]), pk(hi[0], hi[1]), pk(hi[2], hi[3])};
      if (layer == 0) {
        __builtin_amdgcn_raw_buffer_store_b128(o, rs, WS_XT2 + (64 * cp + 8 * w + (lane >> 3)) * 4096 + rp * 128 + blk * 16, 0, SC1);
        float v[8], t[8];
#pragma unroll
        for (int i = 0; i < 4; ++i) { v[2 * i] = bflo(o[i]); v[2 * i + 1] = bfhi(o[i]); }
#pragma unroll
        for (int i = 0; i < 8; ++i) t[i] = swap32add(v[i], v[i] * v[i]);
        float u4[4];
#pragma unroll
        for (int i = 0; i < 4; ++i) u4[i] = dppadd<0x128>(swap16add(t[i], t[i + 4]));
        if ((lane & 8) == 0) {
          const int li = (lane >> 5) * 64 + 8 * blk + 4 * ((lane >> 4) & 1);
#pragma unroll
          for (int i = 0; i < 4; ++i) *(LAS float*)(sm + LD_P2 + (li + i) * 32 + w * 4) = u4[i];
        }
        BAR();
        if (tid < 128) {
          const f32x4 a = *(LAS f32x4*)(sm + LD_P2 + tid * 32), b = *(LAS f32x4*)(sm + LD_P2 + tid * 32 + 16);
          const float tot = ((a[0] + a[1]) + (a[2] + a[3])) + ((b[0] + b[1]) + (b[2] + b[3]));
          const float sc = tot * 0.0625f, fl_ = floorf(sc);
          const unsigned lo32 = ((unsigned)((sc - fl_) * 4294967296.f) & ~15u) | 1u;
          const u64 add = ((u64)(unsigned)(int)fl_ << 32) | lo32;
          __hip_atomic_fetch_add((GAS u64*)(ws + WS_ST2) + (64 * rp + (tid & 63)) * 2 + (tid >> 6), add, __ATOMIC_RELAXED, __HIP_MEMORY_SCOPE_AGENT);
        }
      } else {
        const int row = 8 * w + (lane >> 3);
        *(LAS v4u*)(sm + LD_EX + row * 128 + ((blk ^ (((row >> 1) & 1) << 2)) * 16)) = o;
        BAR();
        const int q4 = (lane & 15) >> 2, gq = lane >> 4, lq = w & 3, drow = 32 * (w >> 2) + 8 * gq + q4;
        LAS unsigned char* tp = sm + LD_EX + drow * 128 + (((lq >> 1) ^ (q4 >> 1)) * 64) + (lq & 1) * 32 + 8 * (lane & 3);
        const v2u a = trread(tp), b = trread(tp + 512);
        *(v4u*)(out + (64 * rp + 16 * lq + (lane & 15)) * D_ + 64 * cp + 32 * (w >> 2) + 8 * gq) = v4u{a.x, a.y, b.x, b.y};
      }
    }
    STAMP(14 + 12 * layer);
    if (layer == 1) break;
    {
      OPQ();
      for (int sp = 0; sp < SPIN_CAP; ++sp) {
        CFENCE();
        const v2u a = __builtin_amdgcn_raw_buffer_load_b64(rs, WS_XT2 + pj * 8192 + tid * 8, 0, SC1);
        const v2u b = __builtin_amdgcn_raw_buffer_load_b64(rs, WS_XT2 + pj * 8192 + 4096 + tid * 8, 0, SC1);
        v4u st[4];
#pragma unroll
        for (int i = 0; i < 4; ++i) st[i] = __builtin_amdgcn_raw_buffer_load_b128(rs, WS_ST2 + tid * 64 + i * 16, 0, SC1);
        bool ok = umax3(umax3(a.x, a.y, b.x), b.y, 0u) != POISON;
        xa[0] = bflo(a.x); xa[1] = bfhi(a.x); xa[2] = bflo(a.y); xa[3] = bfhi(a.y);
        xb[0] = bflo(b.x); xb[1] = bfhi(b.x); xb[2] = bflo(b.y); xb[3] = bfhi(b.y);
#pragma unroll
        for (int i = 0; i < 4; ++i) {
          ok = ok && ((st[i].x & 15u) == 8u) && ((st[i].z & 15u) == 8u);
          const float sum = (float)(int)st[i].y * 16.f + (float)(st[i].x & ~15u) * (1.0f / 268435456.f);
          const float sq = (float)(int)st[i].w * 16.f + (float)(st[i].z & ~15u) * (1.0f / 268435456.f);
          ln_consts(sum, sq, mu[i], rsd[i]);
        }
        if (__all(ok)) break;
      }
    }
  }
}

extern "C" void kernel_launch(void* const* d_in, const int* in_sizes, int n_in, void* d_out, int out_size, void* d_ws, size_t ws_size, hipStream_t stream) {
  (void)in_sizes; (void)n_in; (void)out_size; (void)ws_size;
  hipFuncSetAttribute((const void*)fused_fwd, hipFuncAttributeMaxDynamicSharedMemorySize, LD_END);
  fused_fwd<<<256, 512, LD_END, stream>>>((const u16*)d_in[0], (const u16*)d_in[1], (const u16*)d_in[2], (const u16*)d_in[3], (const u16*)d_in[4],
                                          (const u16*)d_in[5], (const u16*)d_in[6], (const u16*)d_in[8], (const u16*)d_in[9], (const u16*)d_in[10],
                                          (u16*)d_out, (unsigned char*)d_ws);
}
```

```cpp
#include <hip/hip_runtime.h>
#include <stdint.h>

typedef unsigned short u16;
typedef unsigned long long u64;
typedef unsigned v4u __attribute__((ext_vector_type(4)));
typedef unsigned v2u __attribute__((ext_vector_type(2)));
typedef float f32x2 __attribute__((ext_vector_type(2)));
typedef float f32x4 __attribute__((ext_vector_type(4)));
typedef float f32x16 __attribute__((ext_vector_type(16)));
typedef __bf16 bf16x8 __attribute__((ext_vector_type(8)));
typedef __bf16 bf16x2 __attribute__((ext_vector_type(2)));
typedef short s16x4 __attribute__((ext_vector_type(4)));
#define LAS __attribute__((address_space(3)))
#define GAS __attribute__((address_space(1)))
#define DI __device__ __forceinline__

#define EARLY_POISON 1
#define DLY_X 0
#define DLY_H 14
#define DLY_C 10

constexpr int L_ = 2048, D_ = 512, N_ = 128, F_ = 1792;
constexpr int WS_EPOCH = 0;
constexpr int WS_ST0 = 4096;
constexpr int WS_WRDY = 20480;
constexpr int WS_ST2 = 32768;
constexpr int WS_W1F = 65536;
constexpr int WS_W2F = WS_W1F + 1792 * 1024;
constexpr int WS_XT1 = WS_W2F + 1792 * 1024;
constexpr int WS_XT2 = WS_XT1 + (2 << 20);
constexpr int WS_XT3 = WS_XT2 + (2 << 20);
constexpr int WS_H1 = WS_XT3 + (2 << 20);
constexpr int H_BYTES = 32 * 2 * 112 * 1024;
constexpr int WS_H2 = WS_H1 + H_BYTES;
constexpr int WS_XG = WS_H2 + H_BYTES;
constexpr int WS_END = WS_XG + (4 << 20);
constexpr int WS_DBG = 32 << 20;
constexpr int KCS = 4160;
constexpr int LD_K = 0;
constexpr int LD_XT = 33280;
constexpr int LD_F = LD_XT + 65536;
constexpr int LD_U = LD_F + 4096;
constexpr int LD_X = LD_U + 2 * 4608;
constexpr int LD_EX = LD_X + 2 * 4096;
constexpr int STB = 4864;
constexpr int LD_ST = LD_EX + 16384;
constexpr int LD_P1 = LD_ST + 2 * STB;
constexpr int LD_P2 = LD_P1 + 2048;
constexpr int LD_CB = LD_P2 + 4096;
constexpr int LD_FL = LD_CB + 4608;
constexpr int LD_END = LD_FL + 64;
static_assert(LD_END <= 163840, "lds");
constexpr int TROW = 528;
constexpr unsigned POISON = 0xFFFFFFFFu;
constexpr int SPIN_CAP = 1 << 14;
constexpr int SC1 = 16;

#if defined(__HIP_DEVICE_COMPILE__)
#pragma clang attribute push(__attribute__((target("no-packed-fp32-ops"))), apply_to = function)
#endif
DI float u2f(unsigned x) { return __builtin_bit_cast(float, x); }
DI unsigned f2u(float x) { return __builtin_bit_cast(unsigned, x); }
DI unsigned umax(unsigned a, unsigned b) { return a > b ? a : b; }
DI int imin(int a, int b) { return a < b ? a : b; }
DI bool wall(bool p) { return __builtin_amdgcn_ballot_w64(!p) == 0; }
DI bool wany(bool p) { return __builtin_amdgcn_ballot_w64(p) != 0; }
DI float fexp(float x) { return __builtin_amdgcn_exp2f(x * 1.44269504088896f); }
DI float bf2f(unsigned b) { return u2f(b << 16); }
DI float bflo(unsigned p) { return u2f(p << 16); }
DI float bfhi(unsigned p) { return u2f(p & 0xffff0000u); }
DI unsigned pk(float a, float b) { f32x2 f = {a, b}; return __builtin_bit_cast(unsigned, __builtin_convertvector(f, bf16x2)); }
DI float rbf(float a) { return bflo(pk(a, a)); }
DI unsigned umax3(unsigned a, unsigned b, unsigned c) { return umax(umax(a, b), c); }
DI unsigned pz(unsigned m, v4u v) { return umax3(umax3(m, v.x, v.y), v.z, v.w); }
DI f32x16 mfma32(v4u a, v4u b, f32x16 c) { return __builtin_amdgcn_mfma_f32_32x32x16_bf16(__builtin_bit_cast(bf16x8, a), __builtin_bit_cast(bf16x8, b), c, 0, 0, 0); }
DI f32x4 mfma16(v4u a, v4u b, f32x4 c) { return __builtin_amdgcn_mfma_f32_16x16x32_bf16(__builtin_bit_cast(bf16x8, a), __builtin_bit_cast(bf16x8, b), c, 0, 0, 0); }
DI v2u trread(LAS unsigned char* p) { return __builtin_bit_cast(v2u, __builtin_amdgcn_ds_read_tr16_b64_v4i16((LAS s16x4*)p)); }
DI v4u trfrag(LAS unsigned char* p, int d) { const v2u lo = trread(p), hi = trread(p + d); return v4u{lo.x, lo.y, hi.x, hi.y}; }
DI float dot2(unsigned a, unsigned b, float c) { return __builtin_amdgcn_fdot2_f32_bf16(__builtin_bit_cast(bf16x2, a), __builtin_bit_cast(bf16x2, b), c, false); }
#define CFENCE() asm volatile("" ::: "memory")
#define UNDEF(x) asm volatile("" : "=v"(x))
#define SB() __builtin_amdgcn_sched_barrier(0)
#define BAR() do { asm volatile("s_waitcnt lgkmcnt(0)" ::: "memory"); __builtin_amdgcn_s_barrier(); asm volatile("" ::: "memory"); } while (0)
#define DRAIN() asm volatile("s_waitcnt vmcnt(0)" ::: "memory")
#define LD128(vo, so) __builtin_amdgcn_raw_buffer_load_b128(rs, (vo), (so), SC1)
#define ST128(v, vo, so) __builtin_amdgcn_raw_buffer_store_b128((v), rs, (vo) + (so), 0, SC1)
template <int CTRL> DI unsigned dppmov(unsigned x) { return __builtin_amdgcn_update_dpp(0u, x, CTRL, 0xf, 0xf, false); }
template <int CTRL> DI float dppadd(float x) { return x + u2f(dppmov<CTRL>(f2u(x))); }
DI float swap32add(float a, float b) { auto r = __builtin_amdgcn_permlane32_swap(f2u(a), f2u(b), false, false); return u2f(r[0]) + u2f(r[1]); }
DI float swap16add(float a, float b) { auto r = __builtin_amdgcn_permlane16_swap(f2u(a), f2u(b), false, false); return u2f(r[0]) + u2f(r[1]); }
struct cpx { float re, im; };
DI cpx cmul(cpx a, cpx b) { return {a.re * b.re - a.im * b.im, a.re * b.im + a.im * b.re}; }
DI cpx cexp_(float ar2, float rev, float p) {
  float e = __builtin_amdgcn_exp2f(ar2 * p);
  float t = __builtin_amdgcn_fractf(rev * p);
  return {e * __builtin_amdgcn_cosf(t), e * __builtin_amdgcn_sinf(t)};
}
DI void ln_consts(float sum, float sq, float& m, float& s) {
  float mean = sum * (1.0f / D_);
  float var = __builtin_fmaxf(sq * (1.0f / D_) - mean * mean, 0.f);
  m = rbf(mean);
  s = rbf(__builtin_amdgcn_sqrtf(rbf(rbf(var) + 1e-5f)));
}
DI int stab(int p) { return 2 * p + 4 * (p >> 6); }

template <int GRP> DI void conv_loop(LAS unsigned char* fp, LAS unsigned char* gp, f32x4 (&acc)[4], v4u (&W)[32], __amdgpu_buffer_rsrc_t rs, int wv, int wb, bool pf) {
  constexpr int C0 = GRP ? 1 : 0, C1 = GRP ? 2 : 3, C2 = GRP ? 5 : 4, C3 = GRP ? 6 : 7;
#pragma unroll
  for (int r4 = 0; r4 < 4; ++r4) {
    v4u Fr[C3 + 1], Gr[C3 + 1];
#pragma unroll
    for (int p = 0; p <= C3; ++p) {
      const v2u lo = *(volatile LAS v2u*)(fp + 512 * (7 - p) + 192 - 64 * r4), hi = *(volatile LAS v2u*)(fp + 512 * (7 - p) + 200 - 64 * r4);
      Fr[p] = v4u{lo.x, lo.y, hi.x, hi.y};
      Gr[p] = *(LAS v4u*)(gp + 512 * p + 192 - 64 * r4);
    }
    if (pf) {
#pragma unroll
      for (int i = 0; i < 4; ++i) W[8 + 4 * r4 + i] = LD128(wv + i * 1024, wb + (2 + r4) * 4096);
    } else {
#pragma unroll
      for (int i = 0; i < 4; ++i) UNDEF(W[8 + 4 * r4 + i]);
    }
    SB();
#pragma unroll
    for (int m = 0; m <= C3; ++m) {
#pragma unroll
      for (int o = 0; o <= m; ++o) {
        const int c = m + o;
        const int a = c == C0 ? 0 : c == C1 ? 1 : c == C2 ? 2 : c == C3 ? 3 : -1;
        if (a >= 0) {
          acc[a] = mfma16(Fr[m], Gr[o], acc[a]);
          if (o != m) acc[a] = mfma16(Fr[o], Gr[m], acc[a]);
        }
      }
    }
    SB();
  }
}

DI void stage_conv(LAS unsigned char* sm, int tid, const float (&xa)[4], const float (&xb)[4], const float (&mu)[4], const float (&rsd)[4]) {
  float ua[4], ub[4];
#pragma unroll
  for (int i = 0; i < 4; ++i) { const float r = __builtin_amdgcn_rcpf(rsd[i]); ua[i] = rbf(xa[i] - mu[i]) * r; ub[i] = rbf(xb[i] - mu[i]) * r; }
  *(LAS v2u*)(sm + LD_U + 512 + tid * 8) = v2u{pk(ua[0], ua[1]), pk(ua[2], ua[3])};
  *(LAS v2u*)(sm + LD_U + 4608 + 512 + tid * 8) = v2u{pk(ub[0], ub[1]), pk(ub[2], ub[3])};
  *(LAS v2u*)(sm + LD_X + tid * 8) = v2u{pk(xa[0], xa[1]), pk(xa[2], xa[3])};
  *(LAS v2u*)(sm + LD_X + 4096 + tid * 8) = v2u{pk(xb[0], xb[1]), pk(xb[2], xb[3])};
}

#define STAMP(i)
#define CSTAMP(i)
#define FLUSH_STAMPS()
#define SIMDID()
#define COUNT(i)

__global__ void __launch_bounds__(512, 2)
fused_fwd(const u16* __restrict__ x0, const u16* __restrict__ lar, const u16* __restrict__ aim, const u16* __restrict__ crp,
          const u16* __restrict__ cip, const u16* __restrict__ ldt, const u16* __restrict__ dsk, const u16* __restrict__ w1,
          const u16* __restrict__ b1, const u16* __restrict__ w2, u16* __restrict__ out, unsigned char* ws) {
  extern __shared__ __attribute__((aligned(16))) unsigned char smem_[];
  LAS unsigned char* sm = (LAS unsigned char*)smem_;
  int tid = __builtin_amdgcn_workitem_id_x(), lane = tid & 63;
  const int w = __builtin_amdgcn_readfirstlane(tid >> 6);
#define OPQ() do { asm volatile("" : "+v"(tid)); lane = tid & 63; } while (0)
  const int bid = __builtin_amdgcn_workgroup_id_x(), xcd = bid & 7, slot = bid >> 3;
  const int rp = xcd * 4 + (slot >> 3), cp = slot & 7, pj = xcd * 32 + slot;
  const __amdgpu_buffer_rsrc_t rs = __builtin_amdgcn_make_buffer_rsrc((void*)ws, 0, WS_END, 0x00020000);
  const v4u PZ4 = {POISON, POISON, POISON, POISON};
  STAMP(0); CSTAMP(62);

  const int kch = w >> 2, role = ((w >> 1) ^ (w >> 2)) & 1, mode_n = (w & 1) * 64 + lane, dk = 2 * pj + kch;
  const unsigned epoch = __builtin_amdgcn_raw_buffer_load_b32(rs, WS_EPOCH, 0, SC1);
  const v4u xr = *(const v4u*)(x0 + (bid * 8 + w) * D_ + lane * 8);
  const v4u xc = *(const v4u*)(x0 + (64 * rp + lane) * D_ + 64 * cp + 8 * w);
  const unsigned r_lar = lar[dk * N_ + mode_n], r_aim = aim[dk * N_ + mode_n], r_cr = crp[dk * N_ + mode_n], r_ci = cip[dk * N_ + mode_n];
  const unsigned r_ldt = ldt[dk];
  const int wq = w < 7 ? w : 6;
  const int ksg = (bid * 7 + wq) >> 4, dtile = (bid * 7 + wq) & 15;
  const v4u g1 = *(const v4u*)(w1 + (16 * (bid >> 3) + (lane >> 2)) * F_ + 224 * (bid & 7) + 32 * wq + 8 * (lane & 3));
  const v4u g2 = *(const v4u*)(w2 + (16 * ksg + (lane >> 2)) * D_ + 32 * dtile + 8 * (lane & 3));
  const unsigned r_dsk = *(const unsigned*)(dsk + 2 * pj);
  const unsigned r_b1 = b1[224 * cp + imin(tid, 223)];
  ST128(PZ4, tid * 16, WS_XT1 + bid * 8192);
  if (tid < 64) *(LAS v4u*)(sm + LD_U + (tid >> 5) * 4608 + (tid & 31) * 16) = v4u{0, 0, 0, 0};
  if (tid < 16) *(LAS unsigned*)(sm + LD_FL + tid * 4) = 0u;
  int kidx[11], sidx[4];
  {
    const int y0 = 16 + 8 * (tid & 255);
#pragma unroll
    for (int m = 0; m < 11; ++m) kidx[m] = LD_ST + (tid >> 8) * STB + stab((int)rbf((float)imin(2067 - y0 - m, 2048)));
    const int rt = (w >> 1) & 1, g = lane >> 4, j8 = 8 * (16 * (w & 1) + (lane & 15));
#pragma unroll
    for (int reg = 0; reg < 4; ++reg) {
      int e;
      if (rt == 0) e = g == 0 ? reg : g == 1 ? 4 + reg : g == 2 ? 256 + 2 * reg : 512 + 256 * (reg >> 1) + 4 * (reg & 1);
      else e = g == 0 ? 1024 + 256 * reg : (g == 1 && reg == 0) ? 2048 : 2049;
      sidx[reg] = LD_ST + kch * STB + stab(e + j8);
    }
#pragma unroll
    for (int m = 0; m < 11; ++m) asm volatile("" : "+v"(kidx[m]));
#pragma unroll
    for (int m = 0; m < 4; ++m) asm volatile("" : "+v"(sidx[m]));
  }
  SB();
  const unsigned tag = epoch * 0x9E3779B1u + 0x7F4A7C15u;
  {
    float s = 0.f, q = 0.f;
#pragma unroll
    for (int i = 0; i < 4; ++i) { s = dot2(xr[i], 0x3f803f80u, s); q = dot2(xr[i], xr[i], q); }
    float t = swap32add(s, q);
    t = dppadd<0xB1>(t); t = dppadd<0x4E>(t); t = dppadd<0x141>(t); t = dppadd<0x140>(t);
    t = swap16add(t, t);
    const float sum = u2f(__builtin_amdgcn_readlane(f2u(t), 0));
    const float sq = u2f(__builtin_amdgcn_readlane(f2u(t), 32));
    float m, sd; ln_consts(sum, sq, m, sd);
    const unsigned gran = (f2u(m) >> 16) | (f2u(sd) & 0xffff0000u);
    const bool odd = lane & 1;
    const unsigned o0 = dppmov<0xB1>(odd ? xc.x : xc.z), o1 = dppmov<0xB1>(odd ? xc.y : xc.w);
    const unsigned m0 = odd ? xc.z : xc.x, m1 = odd ? xc.w : xc.y;
    const int xo = (4 * w + 2 * (lane & 1)) * 16384 + (lane >> 1) * 16;
    ST128((v4u{odd ? o0 : m0, tag, odd ? m0 : o0, tag}), xo, WS_XG + cp * (32 * 16384) + rp * 512);
    ST128((v4u{odd ? o1 : m1, tag, odd ? m1 : o1, tag}), xo + 16384, WS_XG + cp * (32 * 16384) + rp * 512);
    asm volatile("s_waitcnt vmcnt(2)" ::: "memory");
    if (lane == 0) __builtin_amdgcn_raw_buffer_store_b64(v2u{gran, tag}, rs, 0, WS_ST0 + (bid * 8 + w) * 8, SC1);
  }
  STAMP(1);
  {
    LAS unsigned char* wt = sm + LD_EX + w * 2048;
    *(LAS v4u*)(wt + lane * 16) = g1;
    *(LAS v4u*)(wt + 1024 + lane * 16) = g2;
    const int h = lane >> 5, q = (lane & 15) >> 2, co = ((lane >> 4) & 1) * 32 + 8 * (lane & 3);
    const v4u a = trfrag(wt + (8 * h + q) * 64 + co, 256);
    const v4u b = trfrag(wt + 1024 + (4 * h + q) * 64 + co, 512);
    const int c2 = ksg / 14, rem = ksg - 14 * c2;
    const int sl = rem < 8 ? 16 * (rem >> 1) + 2 * c2 + (rem & 1) : 64 + 6 * c2 + (rem - 8);
    ST128(a, lane * 16, WS_W1F + (((bid & 7) * 7 + wq) * 32 + (bid >> 3)) * 1024);
    ST128(b, lane * 16, WS_W2F + (((dtile >> 1) * 112 + sl) * 2 + (dtile & 1)) * 1024);
    if (tid < 224) *(LAS float*)(sm + LD_CB + 3584 + tid * 4) = bf2f(r_b1);
  }
  STAMP(2);
  ST128(PZ4, tid * 16, WS_H1 + bid * 28672);
  {
    const float dt = rbf(fexp(bf2f(r_ldt)));
    const float ar = -rbf(fexp(bf2f(r_lar))) * dt, ai = bf2f(r_aim) * dt;
    const float ar2 = ar * 1.44269504088896f, rev = ai * 0.159154943091895f;
    LAS unsigned char* tb = sm + LD_XT + (kch * 2 + role) * (32 * TROW) + mode_n * 4;
    ST128(PZ4, tid * 16, WS_H1 + bid * 28672 + 8192);
#define PUTA(row, v) *(LAS unsigned*)(tb + (row) * TROW) = pk((v).re, -(v).im)
#define PUTB(row, v) *(LAS unsigned*)(tb + (row) * TROW) = pk((v).re, (v).im)
    if (role == 0) {
      const cpx e1 = cexp_(ar2, rev, 1.f), e2 = cexp_(ar2, rev, 256.f);
      const cpx p2 = cmul(e1, e1), p3 = cmul(p2, e1), p4 = cmul(p2, p2), p5 = cmul(p4, e1), p6 = cmul(p4, p2), p7 = cmul(p4, p3);
      const cpx W2 = cmul(e2, e2), W3 = cmul(W2, e2), W4 = cmul(W2, W2);
      *(LAS unsigned*)(tb) = 0x00003f80u;
      PUTA(1, e1); PUTA(2, p2); PUTA(3, p3); PUTA(4, p4); PUTA(5, p5); PUTA(6, p6); PUTA(7, p7);
      PUTA(8, e2); PUTA(9, cmul(e2, p2)); PUTA(10, cmul(e2, p4)); PUTA(11, cmul(e2, p6));
      PUTA(12, W2); PUTA(13, cmul(W2, p4)); PUTA(14, W3); PUTA(15, cmul(W3, p4));
      PUTA(16, W4); PUTA(17, cmul(W4, e2)); PUTA(18, cmul(W4, W2)); PUTA(19, cmul(W4, W3)); PUTA(20, cmul(W4, W4));
    } else {
      const cpx e1 = cexp_(ar2, rev, 8.f), e2 = cpx{2.f * bf2f(r_cr) * dt, 2.f * bf2f(r_ci) * dt};
      const cpx q2 = cmul(e1, e1), q4 = cmul(q2, q2);
      cpx b0 = e2, b1_ = cmul(e2, e1), b2 = cmul(e2, q2), b3 = cmul(b1_, q2);
#pragma unroll 1
      for (int a = 0; a < 8; ++a) {
        PUTB(0, b0); PUTB(1, b1_); PUTB(2, b2); PUTB(3, b3);
        b0 = cmul(b0, q4); b1_ = cmul(b1_, q4); b2 = cmul(b2, q4); b3 = cmul(b3, q4);
        tb += 4 * TROW;
      }
    }
  }
  STAMP(3);
  asm volatile("s_waitcnt vmcnt(2)" ::: "memory");
  if (lane == 0 && w < 7) __builtin_amdgcn_raw_buffer_store_b32(tag, rs, 0, WS_WRDY + (((bid & 7) * 7 + w) * 32 + (bid >> 3)) * 4, SC1);
  ST128(PZ4, tid * 16, WS_H1 + bid * 28672 + 16384);
  BAR();
  STAMP(4);
  ST128(PZ4, (tid & 255) * 16, tid < 256 ? WS_H1 + bid * 28672 + 24576 : WS_XT2 + bid * 8192);
  {
    const int rt = (w >> 1) & 1, ct = w & 1, i16 = lane & 15, g = lane >> 4;
    LAS unsigned char* ta = sm + LD_XT + (kch * 2) * (32 * TROW) + (16 * rt + i16) * TROW + g * 16;
    LAS unsigned char* tbp = sm + LD_XT + (kch * 2 + 1) * (32 * TROW) + (16 * ct + i16) * TROW + g * 16;
    v4u fa[8], fb[8];
#pragma unroll
    for (int ks = 0; ks < 8; ++ks) { fa[ks] = *(LAS v4u*)(ta + ks * 64); fb[ks] = *(LAS v4u*)(tbp + ks * 64); }
    SB();
    f32x4 acc = {0.f, 0.f, 0.f, 0.f};
#pragma unroll
    for (int ks = 0; ks < 8; ++ks) acc = mfma16(fa[ks], fb[ks], acc);
#pragma unroll
    for (int reg = 0; reg < 4; ++reg) *(LAS u16*)(sm + sidx[reg]) = (u16)pk(acc[reg], acc[reg]);
  }
  if (tid < 256) ST128(PZ4, tid * 16, WS_XT2 + bid * 8192 + 4096);
  else if (tid < 264) ST128((v4u{0, 0, 0, 0}), (tid - 256) * 16, WS_ST2 + bid * 128);
  v4u ga = LD128(tid * 32, WS_ST0), gb = LD128(tid * 32 + 16, WS_ST0), gc = LD128(tid * 32, WS_XG + pj * 16384), gd = LD128(tid * 32 + 16, WS_XG + pj * 16384);
  unsigned gf = __builtin_amdgcn_raw_buffer_load_b32(rs, (lane & 31) * 4, WS_WRDY + (cp * 7 + wq) * 128, SC1);
  BAR();
  STAMP(5);
  {
    unsigned kv[11];
#pragma unroll
    for (int m = 0; m < 11; ++m) kv[m] = *(LAS u16*)(sm + kidx[m]);
    unsigned k0[4];
#pragma unroll
    for (int m = 0; m < 4; ++m) k0[m] = *(LAS u16*)(sm + LD_ST + (tid & 1) * STB + 2 * m);
    SB();
#pragma unroll
    for (int s = 0; s < 4; ++s) {
      v4u o;
#pragma unroll
      for (int e = 0; e < 4; ++e) o[e] = kv[3 - s + 2 * e] | (kv[4 - s + 2 * e] << 16);
      *(LAS v4u*)(sm + LD_K + ((tid >> 8) * 4 + s) * KCS + 32 + 16 * (tid & 255)) = o;
    }
    if (tid < 2) {
      LAS unsigned char* kp = sm + LD_K + tid * 4 * KCS + 2 * 2064;
      *(LAS v4u*)(kp) = v4u{k0[0], 0, 0, 0};
      *(LAS v4u*)(kp + KCS) = v4u{k0[1] | (k0[0] << 16), 0, 0, 0};
      *(LAS v4u*)(kp + 2 * KCS) = v4u{k0[2] | (k0[1] << 16), k0[0], 0, 0};
      *(LAS v4u*)(kp + 3 * KCS) = v4u{k0[3] | (k0[2] << 16), k0[1] | (k0[0] << 16), 0, 0};
#pragma unroll
      for (int s = 0; s < 4; ++s) *(LAS v4u*)(kp + s * KCS + 16) = v4u{0, 0, 0, 0};
    }
  }
  STAMP(6);
  v4u W[32];
  const int w1b = WS_W1F + (cp * 7 + wq) * 32768;
  {
#pragma unroll 1
    for (int sp = 0; sp < SPIN_CAP; ++sp) {
      const bool ok = ga.y == tag && ga.w == tag && gb.y == tag && gb.w == tag && gc.y == tag && gc.w == tag && gd.y == tag && gd.w == tag && gf == tag;
      if (wall(ok)) break;
      COUNT(60);
      __builtin_amdgcn_s_sleep(2);
      CFENCE();
      ga = LD128(tid * 32, WS_ST0); gb = LD128(tid * 32 + 16, WS_ST0); gc = LD128(tid * 32, WS_XG + pj * 16384); gd = LD128(tid * 32 + 16, WS_XG + pj * 16384);
      gf = __builtin_amdgcn_raw_buffer_load_b32(rs, (lane & 31) * 4, WS_WRDY + (cp * 7 + wq) * 128, SC1);
    }
    STAMP(7);
    float xa[4], xb[4], mu[4], rsd[4];
    mu[0] = bflo(ga.x); mu[1] = bflo(ga.z); mu[2] = bflo(gb.x); mu[3] = bflo(gb.z);
    rsd[0] = bfhi(ga.x); rsd[1] = bfhi(ga.z); rsd[2] = bfhi(gb.x); rsd[3] = bfhi(gb.z);
    xa[0] = bflo(gc.x); xa[1] = bflo(gc.z); xa[2] = bflo(gd.x); xa[3] = bflo(gd.z);
    xb[0] = bfhi(gc.x); xb[1] = bfhi(gc.z); xb[2] = bfhi(gd.x); xb[3] = bfhi(gd.z);
    stage_conv(sm, tid, xa, xb, mu, rsd);
    CFENCE();
    if (w < 7) {
#pragma unroll
      for (int i = 0; i < 8; ++i) W[i] = LD128(lane * 16 + (i & 3) * 1024, w1b + (i >> 2) * 4096);
    } else {
#pragma unroll
      for (int i = 0; i < 8; ++i) UNDEF(W[i]);
    }
  }

#pragma unroll 1
  for (int layer = 0; layer < 2; ++layer) {
    STAMP(8 + 24 * layer);
    OPQ();
    BAR();
    if (layer == 0 && bid == 0 && tid == 0) __builtin_amdgcn_raw_buffer_store_b32(tag, rs, 0, WS_EPOCH, SC1);
    STAMP(9 + 24 * layer);
    OPQ();
    {
      const int ch = w >> 2, hr = (w >> 1) & 1, grp = w & 1, b = lane & 15, g = lane >> 4;
      f32x4 acc[4];
#pragma unroll
      for (int i = 0; i < 4; ++i) acc[i] = f32x4{0.f, 0.f, 0.f, 0.f};
      LAS unsigned char* fp = sm + LD_K + (ch * 4 + (b & 3)) * KCS + 4096 + 16 * g - 2 * (b & ~3) - 3584 - 256 * hr - 192;
      LAS unsigned char* gp = sm + LD_U + ch * 4608 + 480 + 32 * b + 16 * g - 256 * hr - 192;
      if (grp == 0) conv_loop<0>(fp, gp, acc, W, rs, lane * 16, w1b, true); else conv_loop<1>(fp, gp, acc, W, rs, lane * 16, w1b, w < 7);
      STAMP(10 + 24 * layer);
      f32x4 k0 = hr ? acc[2] : acc[0], k1 = hr ? acc[3] : acc[1];
      const f32x4 s0 = hr ? acc[0] : acc[2], s1 = hr ? acc[1] : acc[3];
      *(LAS f32x4*)(sm + LD_EX + w * 2048 + lane * 16) = s0;
      *(LAS f32x4*)(sm + LD_EX + w * 2048 + 1024 + lane * 16) = s1;
      const int cA = grp ? (hr ? 5 : 1) : (hr ? 4 : 0), cB = grp ? (hr ? 6 : 2) : (hr ? 7 : 3);
      const int po = 32 * b + 8 * g;
      const v2u uA = *(LAS v2u*)(sm + LD_U + ch * 4608 + 512 + 512 * cA + po), uB = *(LAS v2u*)(sm + LD_U + ch * 4608 + 512 + 512 * cB + po);
      const v2u xA = *(LAS v2u*)(sm + LD_X + ch * 4096 + 512 * cA + po), xB = *(LAS v2u*)(sm + LD_X + ch * 4096 + 512 * cB + po);
      const float ds = ch ? bfhi(r_dsk) : bflo(r_dsk);
      k0 += f32x4{bflo(xA.x) + bflo(uA.x) * ds, bfhi(xA.x) + bfhi(uA.x) * ds, bflo(xA.y) + bflo(uA.y) * ds, bfhi(xA.y) + bfhi(uA.y) * ds};
      k1 += f32x4{bflo(xB.x) + bflo(uB.x) * ds, bfhi(xB.x) + bfhi(uB.x) * ds, bflo(xB.y) + bflo(uB.y) * ds, bfhi(xB.y) + bfhi(uB.y) * ds};
      if (w < 7) asm volatile("s_waitcnt vmcnt(16)" ::: "memory"); else DRAIN();
      BAR();
      k0 += *(LAS f32x4*)(sm + LD_EX + (w ^ 2) * 2048 + lane * 16);
      k1 += *(LAS f32x4*)(sm + LD_EX + (w ^ 2) * 2048 + 1024 + lane * 16);
      const auto t0 = __builtin_amdgcn_permlane16_swap(pk(k0[0], k0[1]), pk(k1[0], k1[1]), false, false);
      const auto t1 = __builtin_amdgcn_permlane16_swap(pk(k0[2], k0[3]), pk(k1[2], k1[3]), false, false);
      const v4u o = {t0[0], t1[0], t0[1], t1[1]};
      const int cS = (g & 1) ? cB : cA;
      ST128(o, 512 * cS + 32 * b + 16 * (g >> 1), (layer ? WS_XT3 : WS_XT1) + (2 * pj + ch) * 4096);
    }
    STAMP(11 + 24 * layer);
    OPQ();
    if (w < 7) {
#pragma unroll
      for (int i = 24; i < 32; ++i) W[i] = LD128(lane * 16 + (i & 3) * 1024, w1b + (i >> 2) * 4096);
    } else {
#pragma unroll
      for (int i = 24; i < 32; ++i) UNDEF(W[i]);
    }
    if (DLY_X) __builtin_amdgcn_s_sleep(DLY_X);
    {
      v4u xv[8];
      const int xo = (tid >> 3) * 4096 + (tid & 7) * 16, xs = (layer ? WS_XT3 : WS_XT1) + rp * 128;
#pragma unroll
      for (int i = 0; i < 8; ++i) xv[i] = LD128(xo, xs + i * 262144);
      SB();
      if (w < 7 && layer == 0) {
        f32x16 cs;
#pragma unroll
        for (int i = 0; i < 16; ++i) cs[i] = 0.f;
        const v4u ones = {0x3f803f80u, 0x3f803f80u, 0x3f803f80u, 0x3f803f80u};
#pragma unroll
        for (int ks = 0; ks < 32; ++ks) cs = mfma32(ones, W[ks], cs);
        const float bv = *(LAS float*)(sm + LD_CB + 3584 + w * 128 + (lane & 31) * 4);
        const float c0 = rbf(cs[0]), c1 = rbf(cs[0] - c0), c2 = cs[0] - c0 - c1;
        *(LAS v2u*)(sm + LD_CB + w * 512 + lane * 8) = lane < 32 ? v2u{pk(c0, c1), pk(c2, bv)} : v2u{0u, 0u};
      }
      STAMP(12 + 24 * layer);
#pragma unroll 1
      for (int sp = 0; sp < SPIN_CAP; ++sp) {
        unsigned m = 0;
#pragma unroll
        for (int i = 0; i < 8; ++i) m = pz(m, xv[i]);
        if (!wany(m == POISON)) break;
        COUNT(58);
        CFENCE();
#pragma unroll
        for (int i = 0; i < 8; ++i) xv[i] = LD128(xo, xs + i * 262144);
      }
      STAMP(13 + 24 * layer);
      LAS unsigned char* xd = sm + LD_XT + (tid >> 3) * 128 + (((tid & 7) ^ (((tid >> 4) & 1) << 2)) * 16);
#pragma unroll
      for (int i = 0; i < 8; ++i) *(LAS v4u*)(xd + i * 8192) = xv[i];
    }
    BAR();
    STAMP(14 + 24 * layer);
    OPQ();
    const int w2b = WS_W2F + (cp * 112 + (w < 4 ? 16 * w : 64 + 12 * (w - 4))) * 2048;
    const int hbase = (layer ? WS_H2 : WS_H1) + rp * (2 * 112 * 1024);
    {
      const int q4 = (lane & 15) >> 2;
      LAS unsigned char* xp0 = sm + LD_XT + (8 * (lane >> 5) + q4) * 128 + ((lane >> 4) & 1) * 32 + 8 * (lane & 3) + (q4 >> 1) * 64;
      LAS unsigned char* xp1 = sm + LD_XT + (8 * (lane >> 5) + q4) * 128 + ((lane >> 4) & 1) * 32 + 8 * (lane & 3) + (1 - (q4 >> 1)) * 64;
      if (w >= 4) {
#pragma unroll 1
        for (int lt = 0; lt < 2; ++lt) {
          LAS unsigned char* xp = (lt ? xp1 : xp0) + (w - 4) * 16384;
          v4u xf[8];
#pragma unroll
          for (int ks = 0; ks < 8; ++ks) xf[ks] = trfrag(xp + ks * 2048, 512);
          SB();
          float s0 = 0.f, q0 = 0.f, s1 = 0.f, q1 = 0.f;
#pragma unroll
          for (int ks = 0; ks < 8; ++ks) {
#pragma unroll
            for (int e = 0; e < 4; ++e) {
              const float lo = bflo(xf[ks][e]), hi = bfhi(xf[ks][e]);
              s0 += lo; q0 = __builtin_fmaf(lo, lo, q0); s1 += hi; q1 = __builtin_fmaf(hi, hi, q1);
            }
          }
          *(LAS float*)(sm + LD_P1 + lt * 1024 + lane * 16 + (w - 4) * 4) = swap32add(s0 + s1, q0 + q1);
          asm volatile("s_waitcnt lgkmcnt(0)" ::: "memory");
          if (lane == 0) __hip_atomic_fetch_add((LAS unsigned*)(sm + LD_FL + 40 + lt * 4), 1u, __ATOMIC_RELAXED, __HIP_MEMORY_SCOPE_WORKGROUP);
          STAMP(26 + lt + 24 * layer);
        }
        STAMP(15 + 24 * layer);
      }
      int w7 = w;
      asm volatile("" : "+s"(w7));
      if (w7 == 7) {
#pragma unroll
        for (int i = 0; i < 24; ++i) W[i] = LD128(lane * 16 + (i & 3) * 1024, w2b + (i >> 2) * 4096);
        if (layer == 0) {
          v4u pz4 = PZ4;
          asm volatile("" : "+v"(pz4));
#pragma unroll 1
          for (int i = 0; i < 28; ++i) { ST128(pz4, lane * 16, WS_H2 + bid * 28672 + i * 1024); __builtin_amdgcn_s_sleep(1); }
#pragma unroll 1
          for (int i = 0; i < 8; ++i) { ST128(pz4, lane * 16, WS_XT3 + bid * 8192 + i * 1024); __builtin_amdgcn_s_sleep(1); }
        }
      }
      if (w < 7) {
        if (w < 4) __builtin_amdgcn_s_setprio(2); else __builtin_amdgcn_s_setprio(1);
        f32x16 acc[2];
#pragma unroll
        for (int i = 0; i < 16; ++i) { acc[0][i] = 0.f; acc[1][i] = 0.f; }
        constexpr int PD = 3;
        v4u xb0[PD], xb1[PD];
#pragma unroll
        for (int i = 0; i < PD; ++i) { xb0[i] = trfrag(xp0 + i * 2048, 512); xb1[i] = trfrag(xp1 + i * 2048, 512); }
        SB();
#pragma unroll
        for (int ks = 0; ks < 32; ++ks) {
          acc[0] = mfma32(W[ks], xb0[ks % PD], acc[0]);
          SB();
          if (ks + PD < 32) xb0[ks % PD] = trfrag(xp0 + (ks + PD) * 2048, 512);
          SB();
          acc[1] = mfma32(W[ks], xb1[ks % PD], acc[1]);
          SB();
          if (ks + PD < 32) xb1[ks % PD] = trfrag(xp1 + (ks + PD) * 2048, 512);
          if (ks < 24) W[ks] = LD128(lane * 16 + (ks & 3) * 1024, w2b + (ks >> 2) * 4096);
          SB();
        }
        STAMP(16 + 24 * layer);
        if (w < 4) {
#pragma unroll
          for (int i = 24; i < 32; ++i) W[i] = LD128(lane * 16 + (i & 3) * 1024, w2b + (i >> 2) * 4096);
        } else {
#pragma unroll
          for (int i = 24; i < 32; ++i) UNDEF(W[i]);
        }
        if (w < 4) __builtin_amdgcn_s_setprio(0);
        const int hslot = w < 4 ? 16 * w + 2 * cp : 64 + 6 * cp + 2 * (w - 4);
        const v2u cf = *(LAS v2u*)(sm + LD_CB + w * 512 + lane * 8);
#pragma unroll
        for (int lt = 0; lt < 2; ++lt) {
          {
            volatile LAS unsigned* fl = (volatile LAS unsigned*)(sm + LD_FL + 40 + lt * 4);
#pragma unroll 1
            for (int sp = 0; sp < (SPIN_CAP << 6); ++sp) { if (*fl >= (unsigned)(4 * layer + 4)) break; __builtin_amdgcn_s_sleep(1); }
            CFENCE();
          }
          const f32x4 ps = *(LAS f32x4*)(sm + LD_P1 + lt * 1024 + (lane & 31) * 16), pq = *(LAS f32x4*)(sm + LD_P1 + lt * 1024 + 512 + (lane & 31) * 16);
          float m, sd; ln_consts((ps[0] + ps[1]) + (ps[2] + ps[3]), (pq[0] + pq[1]) + (pq[2] + pq[3]), m, sd);
          acc[lt] = mfma32(v4u{cf.x, cf.y, 0u, 0u}, v4u{pk(-m, -m), pk(-m, sd), 0u, 0u}, acc[lt]);
          const float r = 1.44269504088896f * __builtin_amdgcn_rcpf(sd);
#pragma unroll
          for (int s = 0; s < 2; ++s) {
            float h[8], ex[8], d[8], o8[8];
#pragma unroll
            for (int e = 0; e < 8; ++e) h[e] = acc[lt][8 * s + e] * r;
#pragma unroll
            for (int e = 0; e < 8; ++e) ex[e] = __builtin_amdgcn_exp2f(h[e]);
#pragma unroll
            for (int e = 0; e < 8; ++e) d[e] = __builtin_amdgcn_rcpf(__builtin_fmaf(ex[e], ex[e] + 2.f, 2.f));
#pragma unroll
            for (int e = 0; e < 8; ++e) o8[e] = h[e] * __builtin_fmaf(d[e], -1.38629436111989f, 0.693147180559945f);
            ST128((v4u{pk(o8[0], o8[1]), pk(o8[2], o8[3]), pk(o8[4], o8[5]), pk(o8[6], o8[7])}), lane * 16 + s * 1024, hbase + (lt * 112 + hslot) * 1024);
            SB();
          }
        }
        __builtin_amdgcn_s_setprio(0);
      }
    }
    STAMP(17 + 24 * layer);
    OPQ();
    const v4u xres = *(LAS v4u*)(sm + LD_XT + (64 * cp + 8 * w + (lane >> 3)) * 128 + (((lane & 7) ^ (((lane >> 4) & 1) << 2)) * 16));
    {
      const int hs = hbase + (w < 4 ? 16 * w : 64 + 12 * (w - 4)) * 1024;
      asm volatile("s_waitcnt lgkmcnt(0)" ::: "memory");
      if (lane == 0) __hip_atomic_fetch_add((LAS unsigned*)(sm + LD_FL + 36), 1u, __ATOMIC_RELAXED, __HIP_MEMORY_SCOPE_WORKGROUP);
      if (w == 7) {
        volatile LAS unsigned* fl = (volatile LAS unsigned*)(sm + LD_FL + 36);
#pragma unroll 1
        for (int sp = 0; sp < (SPIN_CAP << 6); ++sp) { if (*fl >= (unsigned)(8 * layer + 8)) break; __builtin_amdgcn_s_sleep(2); }
      }
      if (DLY_H) __builtin_amdgcn_s_sleep(DLY_H);
      CFENCE();
      v4u Hb[16];
#pragma unroll
      for (int i = 0; i < 12; ++i) Hb[i] = LD128(lane * 16 + (i & 3) * 1024, hs + (i >> 2) * 4096);
      if (w < 4) {
#pragma unroll
        for (int i = 12; i < 16; ++i) Hb[i] = LD128(lane * 16 + (i & 3) * 1024, hs + (i >> 2) * 4096);
      } else {
#pragma unroll
        for (int i = 12; i < 16; ++i) UNDEF(Hb[i]);
      }
      STAMP(18 + 24 * layer);
      LAS unsigned char* slab = sm + LD_XT + (w & 3) * 16384;
      volatile LAS unsigned* fl = (volatile LAS unsigned*)(sm + LD_FL + (w & 3) * 8);
#pragma unroll
      for (int lt = 0; lt < 2; ++lt) {
        f32x16 a0, a1;
#pragma unroll
        for (int i = 0; i < 16; ++i) { a0[i] = 0.f; a1[i] = 0.f; }
#pragma unroll
        for (int gq = 0; gq < 4; ++gq) {
          if (gq < 3 || w < 4) {
#pragma unroll 1
            for (int sp = 0; sp < SPIN_CAP; ++sp) {
              const unsigned m = pz(pz(pz(pz(0u, Hb[4 * gq]), Hb[4 * gq + 1]), Hb[4 * gq + 2]), Hb[4 * gq + 3]);
              if (!wany(m == POISON)) break;
              COUNT(56 + lt);
              CFENCE();
#pragma unroll
              for (int i = 0; i < 4; ++i) Hb[4 * gq + i] = LD128(lane * 16 + i * 1024, hs + lt * (112 * 1024) + gq * 4096);
            }
#pragma unroll
            for (int i = 0; i < 4; ++i) {
              a0 = mfma32(Hb[4 * gq + i], W[2 * (4 * gq + i)], a0);
              a1 = mfma32(Hb[4 * gq + i], W[2 * (4 * gq + i) + 1], a1);
              SB();
              if (lt == 0) Hb[4 * gq + i] = LD128(lane * 16 + i * 1024, hs + 112 * 1024 + gq * 4096);
              SB();
            }
          }
        }
        STAMP(19 + lt + 24 * layer);
        if (w < 4 && lt == 0) {
          volatile LAS unsigned* f2 = (volatile LAS unsigned*)(sm + LD_FL + 36);
#pragma unroll 1
          for (int sp = 0; sp < (SPIN_CAP << 6); ++sp) { if (*f2 >= (unsigned)(8 * layer + 8)) break; __builtin_amdgcn_s_sleep(1); }
          CFENCE();
        }
        if (w >= 4) {
#pragma unroll 1
          for (int sp = 0; sp < (SPIN_CAP << 6); ++sp) { if (fl[lt] == (unsigned)(layer + 1)) break; __builtin_amdgcn_s_sleep(1); }
          CFENCE();
#pragma unroll
          for (int g = 0; g < 4; ++g) {
            const int so = (lt * 8 + g) * 1024 + ((lane ^ (2 * (lt * 4 + g))) << 4);
            const f32x4 t0 = *(LAS f32x4*)(slab + so), t1 = *(LAS f32x4*)(slab + so + 4096);
#pragma unroll
            for (int e = 0; e < 4; ++e) { a0[4 * g + e] += t0[e]; a1[4 * g + e] += t1[e]; }
          }
        }
#pragma unroll
        for (int g = 0; g < 4; ++g) {
          const int so = (lt * 8 + g) * 1024 + ((lane ^ (2 * (lt * 4 + g))) << 4);
          *(LAS f32x4*)(slab + so) = f32x4{a0[4 * g], a0[4 * g + 1], a0[4 * g + 2], a0[4 * g + 3]};
          *(LAS f32x4*)(slab + so + 4096) = f32x4{a1[4 * g], a1[4 * g + 1], a1[4 * g + 2], a1[4 * g + 3]};
        }
        if (w < 4) { asm volatile("s_waitcnt lgkmcnt(0)" ::: "memory"); if (lane == 0) fl[lt] = (unsigned)(layer + 1); }
      }
    }
    STAMP(21 + 24 * layer);
    if (w == 7) DRAIN();
    BAR();
    STAMP(22 + 24 * layer);
    OPQ();
    {
      const int blk = lane & 7, r = (w & 3) * 8 + (lane >> 3);
      LAS unsigned char* sp = sm + LD_XT + ((blk >> 2) * 8 + (w >> 2) * 4 + (blk & 3)) * 1024 + ((r ^ (2 * blk)) << 4);
      f32x4 pl[4], ph[4];
#pragma unroll
      for (int v = 0; v < 4; ++v) { pl[v] = *(LAS f32x4*)(sp + v * 16384); ph[v] = *(LAS f32x4*)(sp + v * 16384 + 512); }
      SB();
      f32x4 lo = {bflo(xres.x), bfhi(xres.x), bflo(xres.y), bfhi(xres.y)}, hi = {bflo(xres.z), bfhi(xres.z), bflo(xres.w), bfhi(xres.w)};
      lo += (pl[0] + pl[1]) + (pl[2] + pl[3]); hi += (ph[0] + ph[1]) + (ph[2] + ph[3]);
      const v4u o = {pk(lo[0], lo[1]), pk(lo[2], lo[3]), pk(hi[0], hi[1]), pk(hi[2], hi[3])};
      if (layer == 0) {
        ST128(o, (8 * w + (lane >> 3)) * 4096 + blk * 16, WS_XT2 + 64 * cp * 4096 + rp * 128);
        float v[8], t[8];
#pragma unroll
        for (int i = 0; i < 4; ++i) { v[2 * i] = bflo(o[i]); v[2 * i + 1] = bfhi(o[i]); }
#pragma unroll
        for (int i = 0; i < 8; ++i) t[i] = swap32add(v[i], v[i] * v[i]);
        float u4[4];
#pragma unroll
        for (int i = 0; i < 4; ++i) u4[i] = dppadd<0x128>(swap16add(t[i], t[i + 4]));
        if ((lane & 8) == 0) {
          *(LAS f32x4*)(sm + LD_P2 + w * 512 + ((lane >> 5) * 64 + 8 * blk + 4 * ((lane >> 4) & 1)) * 4) = f32x4{u4[0], u4[1], u4[2], u4[3]};
        }
        BAR();
        STAMP(23 + 24 * layer);
        if (tid < 128) {
          float a[8];
#pragma unroll
          for (int i = 0; i < 8; ++i) a[i] = *(LAS float*)(sm + LD_P2 + i * 512 + tid * 4);
          const float tot = ((a[0] + a[1]) + (a[2] + a[3])) + ((a[4] + a[5]) + (a[6] + a[7]));
          const float sc = tot * 0.0625f, fl_ = __builtin_floorf(sc);
          const unsigned lo32 = ((unsigned)((sc - fl_) * 4294967296.f) & ~15u) | 1u;
          const u64 add = ((u64)(unsigned)(int)fl_ << 32) | lo32;
          __hip_atomic_fetch_add((GAS u64*)(ws + WS_ST2) + (tid >> 6) * 2048 + 64 * rp + (tid & 63), add, __ATOMIC_RELAXED, __HIP_MEMORY_SCOPE_AGENT);
        }
      } else {
        const int row = 8 * w + (lane >> 3);
        *(LAS v4u*)(sm + LD_EX + row * 128 + ((blk ^ (((row >> 1) & 1) << 2)) * 16)) = o;
        BAR();
        const int q4 = (lane & 15) >> 2, gq = lane >> 4, lq = w & 3, drow = 32 * (w >> 2) + 8 * gq + q4;
        LAS unsigned char* tp = sm + LD_EX + drow * 128 + (((lq >> 1) ^ (q4 >> 1)) * 64) + (lq & 1) * 32 + 8 * (lane & 3);
        *(v4u*)(out + (64 * rp + 16 * lq + (lane & 15)) * D_ + 64 * cp + 32 * (w >> 2) + 8 * gq) = trfrag(tp, 512);
      }
    }
    STAMP(24 + 24 * layer);
    if (layer == 1) { STAMP(61); CSTAMP(63); SIMDID(); FLUSH_STAMPS(); break; }
    {
      OPQ();
      if (w < 7) {
#pragma unroll
        for (int i = 0; i < 8; ++i) W[i] = LD128(lane * 16 + (i & 3) * 1024, w1b + (i >> 2) * 4096);
      } else {
#pragma unroll
        for (int i = 0; i < 8; ++i) UNDEF(W[i]);
      }
      if (DLY_C) __builtin_amdgcn_s_sleep(DLY_C);
      v2u a, b; v4u st[4];
#pragma unroll 1
      for (int sp = 0; sp < SPIN_CAP; ++sp) {
        CFENCE();
        a = __builtin_amdgcn_raw_buffer_load_b64(rs, tid * 8, WS_XT2 + pj * 8192, SC1);
        b = __builtin_amdgcn_raw_buffer_load_b64(rs, tid * 8, WS_XT2 + pj * 8192 + 4096, SC1);
        st[0] = LD128(tid * 32, WS_ST2); st[1] = LD128(tid * 32 + 16, WS_ST2); st[2] = LD128(tid * 32, WS_ST2 + 16384); st[3] = LD128(tid * 32 + 16, WS_ST2 + 16384);
        bool ok = umax3(umax3(a.x, a.y, b.x), b.y, 0u) != POISON;
#pragma unroll
        for (int i = 0; i < 4; ++i) ok = ok && ((st[i].x & 15u) == 8u) && ((st[i].z & 15u) == 8u);
        if (wall(ok)) break;
        COUNT(59);
        __builtin_amdgcn_s_sleep(4);
      }
      STAMP(25);
      float xa[4], xb[4], mu[4], rsd[4];
      xa[0] = bflo(a.x); xa[1] = bfhi(a.x); xa[2] = bflo(a.y); xa[3] = bfhi(a.y);
      xb[0] = bflo(b.x); xb[1] = bfhi(b.x); xb[2] = bflo(b.y); xb[3] = bfhi(b.y);
#pragma unroll
      for (int i = 0; i < 4; ++i) {
        const unsigned sl = st[i >> 1][2 * (i & 1)], sh = st[i >> 1][2 * (i & 1) + 1], ql = st[2 + (i >> 1)][2 * (i & 1)], qh = st[2 + (i >> 1)][2 * (i & 1) + 1];
        const float sum = (float)(int)sh * 16.f + (float)(sl & ~15u) * (1.0f / 268435456.f);
        const float sq = (float)(int)qh * 16.f + (float)(ql & ~15u) * (1.0f / 268435456.f);
        ln_consts(sum, sq, mu[i], rsd[i]);
      }
      stage_conv(sm, tid, xa, xb, mu, rsd);
    }
  }
}

#if defined(__HIP_DEVICE_COMPILE__)
#pragma clang attribute pop
#endif

extern "C" void kernel_launch(void* const* d_in, const int* in_sizes, int n_in, void* d_out, int out_size, void* d_ws, size_t ws_size, hipStream_t stream) {
  (void)in_sizes; (void)n_in; (void)out_size; (void)ws_size;
  (void)hipFuncSetAttribute((const void*)fused_fwd, hipFuncAttributeMaxDynamicSharedMemorySize, LD_END);
  fused_fwd<<<256, 512, LD_END, stream>>>((const u16*)d_in[0], (const u16*)d_in[1], (const u16*)d_in[2], (const u16*)d_in[3], (const u16*)d_in[4],
                                          (const u16*)d_in[5], (const u16*)d_in[6], (const u16*)d_in[8], (const u16*)d_in[9], (const u16*)d_in[10],
                                          (u16*)d_out, (unsigned char*)d_ws);
}
```

```cpp
#include <hip/hip_runtime.h>
#include <stdint.h>

typedef unsigned short u16;
typedef unsigned long long u64;
typedef unsigned v4u __attribute__((ext_vector_type(4)));
typedef unsigned v2u __attribute__((ext_vector_type(2)));
typedef float f32x2 __attribute__((ext_vector_type(2)));
typedef float f32x4 __attribute__((ext_vector_type(4)));
typedef float f32x8 __attribute__((ext_vector_type(8)));
typedef float f32x16 __attribute__((ext_vector_type(16)));
typedef __bf16 bf16x8 __attribute__((ext_vector_type(8)));
typedef __bf16 bf16x2 __attribute__((ext_vector_type(2)));
typedef short s16x4 __attribute__((ext_vector_type(4)));
#define LAS __attribute__((address_space(3)))
#define GAS __attribute__((address_space(1)))
#define DI __device__ __forceinline__

#define DLY_X 0
#define HSLP 3
#define PZPOS 0
#define NE 12
#define OS1 12
#define OM0 12
#define OA1 4
#define OB0 20
#define OB1 24
#define YA1 4
#define YB0 12
#define YB1 16
#define OA(l) ((l) ? OA1 : 0)
#define OB(l) ((l) ? OB1 : OB0)
#define YA(l) ((l) ? YA1 : 0)
#define YB(l) ((l) ? YB1 : YB0)
#define SPF_BYTES 46000
#define L0W 20
#define NHC 6
#define OGATE 0
#define DLY_H 8
#define DLY_C 4
#define MAPB 2
#define CODE_LO 0
#define CODE_BYTES 0
#define PREW 1

constexpr int D_ = 512, N_ = 128, F_ = 1792;
constexpr int WS_EPOCH = 0;
constexpr int WS_ST0 = 4096;
constexpr int WS_WRDY = 32768;
constexpr int WS_W1F = 65536;
constexpr int WS_W2F = WS_W1F + 1792 * 1024;
constexpr int WS_XT1 = WS_W2F + 1792 * 1024;
constexpr int WS_XT2 = WS_XT1 + (2 << 20);
constexpr int WS_XT3 = WS_XT2 + (2 << 20);
constexpr int WS_H1 = WS_XT3 + (2 << 20);
constexpr int H_BYTES = 32 * 2 * 112 * 1024;
constexpr int WS_H2 = WS_H1 + H_BYTES;
constexpr int WS_XG = WS_H2 + H_BYTES;
constexpr int WS_STP = WS_XG + (4 << 20);
constexpr int WS_END = WS_STP + 65536;
constexpr int WS_DBG = 32 << 20;
constexpr int KCS = 4160;
constexpr int LD_K = 0;
constexpr int LD_U = 33280;
constexpr int LD_X = LD_U + 2 * 4608;
constexpr int LD_WT = LD_X + 2 * 4096;
constexpr int LD_XT = 65536;
constexpr int LD_EX = LD_XT + 65536;
constexpr int STB = 2048;
constexpr int LD_ST = LD_EX + 8192;
constexpr int LD_P1 = LD_ST + 2 * STB;
constexpr int LD_P2 = LD_P1 + 1024;
constexpr int LD_CB = LD_P2 + 4096;
constexpr int LD_FL = LD_CB + 3584;
constexpr int LD_END = LD_FL + 128;
static_assert(LD_WT + 14336 <= LD_XT, "lds");
static_assert(LD_END <= 163840, "lds");
constexpr int TROW = 528;
constexpr unsigned POISON = 0xFFFFFFFFu;
constexpr int SPIN_CAP = 1 << 14;
constexpr int SC1 = 16;

DI float u2f(unsigned x) { return __builtin_bit_cast(float, x); }
DI unsigned f2u(float x) { return __builtin_bit_cast(unsigned, x); }
DI unsigned umax(unsigned a, unsigned b) { return a > b ? a : b; }
DI int imin(int a, int b) { return a < b ? a : b; }
DI bool wall(bool p) { return __builtin_amdgcn_ballot_w64(!p) == 0; }
DI bool wany(bool p) { return __builtin_amdgcn_ballot_w64(p) != 0; }
DI float fexp(float x) { return __builtin_amdgcn_exp2f(x * 1.44269504088896f); }
DI float bflo(unsigned p) { return u2f(p << 16); }
DI float bfhi(unsigned p) { return u2f(p & 0xffff0000u); }
DI unsigned pk(float a, float b) { f32x2 f = {a, b}; return __builtin_bit_cast(unsigned, __builtin_convertvector(f, bf16x2)); }
DI float rbf(float a) { return bflo(pk(a, a)); }
DI unsigned umax3(unsigned a, unsigned b, unsigned c) { return umax(umax(a, b), c); }
DI unsigned pz(unsigned m, v4u v) { return umax3(umax3(m, v.x, v.y), v.z, v.w); }
DI f32x16 mfma32(v4u a, v4u b, f32x16 c) { return __builtin_amdgcn_mfma_f32_32x32x16_bf16(__builtin_bit_cast(bf16x8, a), __builtin_bit_cast(bf16x8, b), c, 0, 0, 0); }
DI f32x4 mfma16(v4u a, v4u b, f32x4 c) { return __builtin_amdgcn_mfma_f32_16x16x32_bf16(__builtin_bit_cast(bf16x8, a), __builtin_bit_cast(bf16x8, b), c, 0, 0, 0); }
DI f32x16 cat4(f32x4 a, f32x4 b, f32x4 c, f32x4 d) {
  const f32x8 lo = __builtin_shufflevector(a, b, 0, 1, 2, 3, 4, 5, 6, 7), hi = __builtin_shufflevector(c, d, 0, 1, 2, 3, 4, 5, 6, 7);
  return __builtin_shufflevector(lo, hi, 0, 1, 2, 3, 4, 5, 6, 7, 8, 9, 10, 11, 12, 13, 14, 15);
}
DI f32x16 zero16() { f32x16 z; for (int i = 0; i < 16; ++i) z[i] = 0.f; return z; }
DI v2u trread(LAS unsigned char* p) { return __builtin_bit_cast(v2u, __builtin_amdgcn_ds_read_tr16_b64_v4i16((LAS s16x4*)p)); }
DI v4u trfrag(LAS unsigned char* p, int d) { const v2u lo = trread(p), hi = trread(p + d); return v4u{lo.x, lo.y, hi.x, hi.y}; }
DI float dot2(unsigned a, unsigned b, float c) { return __builtin_amdgcn_fdot2_f32_bf16(__builtin_bit_cast(bf16x2, a), __builtin_bit_cast(bf16x2, b), c, false); }
#define CFENCE() asm volatile("" ::: "memory")
#define UNDEF(x) asm volatile("" : "=v"(x))
#define SB() __builtin_amdgcn_sched_barrier(0)
#define LGKM0() asm volatile("s_waitcnt lgkmcnt(0)" ::: "memory")
#define BAR() do { LGKM0(); __builtin_amdgcn_s_barrier(); CFENCE(); } while (0)
#define VM0() __builtin_amdgcn_s_waitcnt(0x0F70)
#define DRAIN() asm volatile("s_waitcnt vmcnt(0)" ::: "memory")
#define LD128(vo, so) __builtin_amdgcn_raw_buffer_load_b128(rs, (vo), (so), SC1)
#define ST128(v, vo, so) __builtin_amdgcn_raw_buffer_store_b128((v), rs, (vo) + (so), 0, SC1)
#define LDW(lo, hi, base) _Pragma("unroll") for (int i_ = (lo); i_ < (hi); ++i_) { W[i_] = LD128(lane * 16 + (i_ & 3) * 1024, (base) + (i_ >> 2) * 4096); SB(); }
#define OPQ() do { asm volatile("" : "+v"(tid)); lane = tid & 63; } while (0)
template <int CTRL> DI unsigned dppmov(unsigned x) { return __builtin_amdgcn_update_dpp(0u, x, CTRL, 0xf, 0xf, false); }
template <int CTRL> DI float dppadd(float x) { return x + u2f(dppmov<CTRL>(f2u(x))); }
DI float swap32add(float a, float b) { auto r = __builtin_amdgcn_permlane32_swap(f2u(a), f2u(b), false, false); return u2f(r[0]) + u2f(r[1]); }
DI float swap16add(float a, float b) { auto r = __builtin_amdgcn_permlane16_swap(f2u(a), f2u(b), false, false); return u2f(r[0]) + u2f(r[1]); }
DI f32x2 cmul(f32x2 a, f32x2 b) { const f32x2 br = {-b.y, b.x}; return __builtin_elementwise_fma(a.yy, br, a.xx * b); }
DI f32x2 cexp_(float ar2, float rev, float p) {
  const float e = __builtin_amdgcn_exp2f(ar2 * p), t = __builtin_amdgcn_fractf(rev * p);
  return f32x2{e * __builtin_amdgcn_cosf(t), e * __builtin_amdgcn_sinf(t)};
}
DI void ln_consts(float mean, float msq, float& m, float& s) {
  const float var = __builtin_fmaxf(__builtin_fmaf(-mean, mean, msq), 0.f);
  m = rbf(mean);
  s = rbf(__builtin_amdgcn_sqrtf(rbf(rbf(var) + 1e-5f)));
}
DI void lds_wait_ge(LAS unsigned char* p, unsigned v) {
#pragma unroll 1
  for (int sp = 0; sp < (SPIN_CAP << 6); ++sp) { if (*(volatile LAS unsigned*)p >= v) break; __builtin_amdgcn_s_sleep(1); }
  CFENCE();
}
DI void lds_add1(LAS unsigned char* p, int) {
  asm volatile("s_mov_b64 exec, 1\n\tds_add_u32 %0, %1\n\ts_mov_b64 exec, -1" :: "v"((unsigned)(size_t)p), "v"(1u) : "memory");
}
DI void stage_conv(LAS unsigned char* sm, int p2, v2u xa, v2u xb, const float (&r)[4], const float (&nb)[4]) {
  *(LAS v2u*)(sm + LD_X + p2) = xa;
  *(LAS v2u*)(sm + LD_X + 4096 + p2) = xb;
  *(LAS v2u*)(sm + LD_U + 512 + p2) = v2u{pk(__builtin_fmaf(bflo(xa.x), r[0], nb[0]), __builtin_fmaf(bfhi(xa.x), r[1], nb[1])), pk(__builtin_fmaf(bflo(xa.y), r[2], nb[2]), __builtin_fmaf(bfhi(xa.y), r[3], nb[3]))};
  *(LAS v2u*)(sm + LD_U + 4608 + 512 + p2) = v2u{pk(__builtin_fmaf(bflo(xb.x), r[0], nb[0]), __builtin_fmaf(bfhi(xb.x), r[1], nb[1])), pk(__builtin_fmaf(bflo(xb.y), r[2], nb[2]), __builtin_fmaf(bfhi(xb.y), r[3], nb[3]))};
}

DI int stage_pos(int tid) { return 4 * tid; }
DI void expand_addr(int tid, int (&kidx)[11]) {
  const int l0 = 2051 - 8 * (tid & 255), base = LD_ST + (tid >> 8) * STB;
  const float f0 = (float)l0;
#pragma unroll
  for (int m = 0; m < 12; m += 2) {
    const unsigned pp = pk(__builtin_fmaxf(f0 - (float)m, 128.f), __builtin_fmaxf(f0 - (float)(m + 1), 128.f)) - 0x42804280u;
    kidx[m] = base + 2 * imin(l0 - m, (int)(pp & 0xffffu));
    if (m + 1 < 11) kidx[m + 1] = base + 2 * imin(l0 - m - 1, (int)(pp >> 16));
  }
}
DI void expand_kernel(LAS unsigned char* sm, int tid, const int (&kidx)[11]) {
    unsigned kv[11];
#pragma unroll
    for (int m = 0; m < 11; ++m) kv[m] = *(LAS u16*)(sm + kidx[m]);
    unsigned k0[4];
#pragma unroll
    for (int m = 0; m < 4; ++m) k0[m] = *(LAS u16*)(sm + LD_ST + (tid & 1) * STB + 2 * m);
    SB();
#pragma unroll
    for (int s = 0; s < 4; ++s) {
      v4u o;
#pragma unroll
      for (int e = 0; e < 4; ++e) o[e] = kv[3 - s + 2 * e] | (kv[4 - s + 2 * e] << 16);
      *(LAS v4u*)(sm + LD_K + ((tid >> 8) * 4 + s) * KCS + 32 + 16 * (tid & 255)) = o;
    }
    if (tid < 2) {
      LAS unsigned char* kp = sm + LD_K + tid * 4 * KCS + 2 * 2064;
      *(LAS v4u*)(kp) = v4u{k0[0], 0, 0, 0};
      *(LAS v4u*)(kp + KCS) = v4u{k0[1] | (k0[0] << 16), 0, 0, 0};
      *(LAS v4u*)(kp + 2 * KCS) = v4u{k0[2] | (k0[1] << 16), k0[0], 0, 0};
      *(LAS v4u*)(kp + 3 * KCS) = v4u{k0[3] | (k0[2] << 16), k0[1] | (k0[0] << 16), 0, 0};
#pragma unroll
      for (int s = 0; s < 4; ++s) *(LAS v4u*)(kp + s * KCS + 16) = v4u{0, 0, 0, 0};
    }
    if (tid >= 64 && tid < 128) *(LAS v4u*)(sm + LD_U + ((tid >> 5) & 1) * 4608 + (tid & 31) * 16) = v4u{0, 0, 0, 0};
}

struct Dbg {};
#define STAMP(i)
#define COUNT(i)
#define FLUSH_STAMPS()

constexpr int C_EXA = LD_FL, C_XB = LD_FL + 4, F_STA = LD_FL + 8, F_STB = LD_FL + 12, C_XDONE = LD_FL + 16, C_SLA = LD_FL + 20, C_FINA = LD_FL + 24,
              C_SLB = LD_FL + 28, C_PUBO = LD_FL + 32, C_PUBY = LD_FL + 36, C_ST = LD_FL + 40;
DI float fadd(float a, float b) { float r = a + b; asm("" : "+v"(r)); return r; }
DI f32x4 add4(f32x4 a, f32x4 b) { return f32x4{fadd(a[0], b[0]), fadd(a[1], b[1]), fadd(a[2], b[2]), fadd(a[3], b[3])}; }

#define CONV_S1() \
    LDFH(0, 0, 0); \
    LDFH(0, 0, 1); \
    LDG(0, 0); \
    LDG(0, 1); \
    LDG(0, 2); \
    LDG(0, 3); \
    LDFH(0, 1, 0); \
    LDFH(0, 1, 1); \
    LDFH(0, 2, 0); \
    LDFH(0, 2, 1); \
    SB(); \
    acc[0] = mfma16(Fr[0][0], Gr[0][0], acc[0]); SB(); LDFH(0, 3, 0); SB(); \
    acc[1] = mfma16(Fr[0][0], Gr[0][1], acc[1]); SB(); LDFH(0, 3, 1); SB(); \
    acc[2] = mfma16(Fr[0][0], Gr[0][2], acc[2]); SB(); LDFH(1, 0, 0); SB(); \
    acc[3] = mfma16(Fr[0][0], Gr[0][3], acc[3]); SB(); LDFH(1, 0, 1); SB(); \
    acc[1] = mfma16(Fr[0][1], Gr[0][0], acc[1]); SB(); LDG(1, 0); SB(); \
    acc[2] = mfma16(Fr[0][1], Gr[0][1], acc[2]); SB(); LDG(1, 1); SB(); \
    acc[3] = mfma16(Fr[0][1], Gr[0][2], acc[3]); SB(); LDG(1, 2); SB(); \
    acc[0] = mfma16(Fr[1][0], Gr[1][0], acc[0]); SB(); LDG(1, 3); SB(); \
    acc[2] = mfma16(Fr[0][2], Gr[0][0], acc[2]); SB(); LDFH(1, 1, 0); SB(); \
    acc[3] = mfma16(Fr[0][2], Gr[0][1], acc[3]); SB(); LDFH(1, 1, 1); SB(); \
    acc[1] = mfma16(Fr[1][0], Gr[1][1], acc[1]); SB(); LDFH(1, 2, 0); SB(); \
    acc[2] = mfma16(Fr[1][0], Gr[1][2], acc[2]); SB(); LDFH(1, 2, 1); SB(); \
    acc[3] = mfma16(Fr[0][3], Gr[0][0], acc[3]); SB(); LDFH(1, 3, 0); SB(); \
    acc[1] = mfma16(Fr[1][1], Gr[1][0], acc[1]); SB(); LDFH(1, 3, 1); SB(); \
    acc[2] = mfma16(Fr[1][1], Gr[1][1], acc[2]); SB(); LDFH(1, 4, 0); SB(); \
    acc[3] = mfma16(Fr[1][0], Gr[1][3], acc[3]); SB(); LDFH(1, 4, 1); SB(); \
    acc[2] = mfma16(Fr[1][2], Gr[1][0], acc[2]); SB(); LDFH(1, 5, 0); SB(); \
    acc[3] = mfma16(Fr[1][1], Gr[1][2], acc[3]); SB(); LDFH(1, 5, 1); SB(); \
    acc[3] = mfma16(Fr[1][2], Gr[1][1], acc[3]); SB(); \
    acc[3] = mfma16(Fr[1][3], Gr[1][0], acc[3]); SB(); \
    do {} while (0)
#define CONV_S2A() \
    SB(); \
    acc[4] = mfma16(Fr[1][1], Gr[1][3], acc[4]); SB(); LDFH(1, 6, 0); SB(); \
    acc[5] = mfma16(Fr[1][2], Gr[1][3], acc[5]); SB(); LDFH(1, 6, 1); SB(); \
    acc[6] = mfma16(Fr[1][3], Gr[1][3], acc[6]); SB(); LDFH(1, 7, 0); SB(); \
    acc[4] = mfma16(Fr[1][2], Gr[1][2], acc[4]); SB(); LDFH(1, 7, 1); SB(); \
    acc[5] = mfma16(Fr[1][3], Gr[1][2], acc[5]); SB(); PZHOOK(0); \
    acc[6] = mfma16(Fr[1][4], Gr[1][2], acc[6]); SB(); \
    acc[4] = mfma16(Fr[1][3], Gr[1][1], acc[4]); SB(); LDFH(0, 4, 0); SB(); \
    acc[5] = mfma16(Fr[1][4], Gr[1][1], acc[5]); SB(); LDFH(0, 4, 1); SB(); \
    acc[7] = mfma16(Fr[1][4], Gr[1][3], acc[7]); SB(); LDFH(0, 5, 0); SB(); \
    acc[4] = mfma16(Fr[1][4], Gr[1][0], acc[4]); SB(); LDFH(0, 5, 1); SB(); \
    acc[5] = mfma16(Fr[1][5], Gr[1][0], acc[5]); SB(); LDFH(0, 6, 0); SB(); \
    acc[6] = mfma16(Fr[1][5], Gr[1][1], acc[6]); SB(); LDFH(0, 6, 1); SB(); \
    acc[7] = mfma16(Fr[1][5], Gr[1][2], acc[7]); SB(); LDFH(0, 7, 0); SB(); PZHOOK(1); \
    acc[6] = mfma16(Fr[1][6], Gr[1][0], acc[6]); SB(); LDFH(0, 7, 1); SB(); \
    acc[7] = mfma16(Fr[1][6], Gr[1][1], acc[7]); SB(); LDFH(0, 3, 0); SB(); \
    acc[7] = mfma16(Fr[1][7], Gr[1][0], acc[7]); SB(); LDFH(0, 3, 1); SB(); \
    do {} while (0)
#define CONV_S2B() \
    acc[4] = mfma16(Fr[0][4], Gr[0][0], acc[4]); SB(); LDFH(0, 2, 0); SB(); \
    acc[5] = mfma16(Fr[0][4], Gr[0][1], acc[5]); SB(); LDFH(0, 2, 1); SB(); \
    acc[6] = mfma16(Fr[0][4], Gr[0][2], acc[6]); SB(); LDFH(0, 1, 0); SB(); \
    acc[7] = mfma16(Fr[0][4], Gr[0][3], acc[7]); SB(); LDFH(0, 1, 1); SB(); \
    acc[5] = mfma16(Fr[0][5], Gr[0][0], acc[5]); SB(); PZHOOK(2); \
    acc[6] = mfma16(Fr[0][5], Gr[0][1], acc[6]); SB(); \
    acc[7] = mfma16(Fr[0][5], Gr[0][2], acc[7]); SB(); \
    acc[4] = mfma16(Fr[0][3], Gr[0][1], acc[4]); SB(); \
    acc[6] = mfma16(Fr[0][6], Gr[0][0], acc[6]); SB(); LDFH(0, 0, 0); SB(); \
    acc[7] = mfma16(Fr[0][6], Gr[0][1], acc[7]); SB(); LDFH(0, 0, 1); SB(); \
    acc[5] = mfma16(Fr[0][3], Gr[0][2], acc[5]); SB(); LDFH(1, 0, 0); SB(); \
    acc[6] = mfma16(Fr[0][3], Gr[0][3], acc[6]); SB(); LDFH(1, 0, 1); SB(); PZHOOK(3); \
    acc[7] = mfma16(Fr[0][7], Gr[0][0], acc[7]); SB(); LDFH(1, 1, 0); SB(); \
    acc[4] = mfma16(Fr[0][2], Gr[0][2], acc[4]); SB(); LDFH(1, 1, 1); SB(); \
    acc[5] = mfma16(Fr[0][2], Gr[0][3], acc[5]); SB(); \
    acc[4] = mfma16(Fr[0][1], Gr[0][3], acc[4]); SB(); \
    do {} while (0)
#define CONV_S3() \
    LDG(0, 4); \
    LDG(0, 5); \
    LDG(0, 6); \
    LDG(0, 7); \
    LDG(1, 4); \
    LDG(1, 5); \
    SB(); \
    acc[4] = mfma16(Fr[0][0], Gr[0][4], acc[4]); SB(); LDG(1, 6); SB(); \
    acc[5] = mfma16(Fr[0][0], Gr[0][5], acc[5]); SB(); LDG(1, 7); SB(); \
    acc[6] = mfma16(Fr[0][0], Gr[0][6], acc[6]); SB(); LDFH(1, 2, 0); SB(); \
    acc[7] = mfma16(Fr[0][0], Gr[0][7], acc[7]); SB(); LDFH(1, 2, 1); SB(); \
    acc[5] = mfma16(Fr[0][1], Gr[0][4], acc[5]); SB(); LDFH(1, 3, 0); SB(); \
    acc[6] = mfma16(Fr[0][1], Gr[0][5], acc[6]); SB(); LDFH(1, 3, 1); SB(); \
    acc[7] = mfma16(Fr[0][1], Gr[0][6], acc[7]); SB(); \
    acc[4] = mfma16(Fr[1][0], Gr[1][4], acc[4]); SB(); \
    acc[6] = mfma16(Fr[0][2], Gr[0][4], acc[6]); SB(); \
    acc[7] = mfma16(Fr[0][2], Gr[0][5], acc[7]); SB(); \
    acc[5] = mfma16(Fr[1][0], Gr[1][5], acc[5]); SB(); \
    acc[6] = mfma16(Fr[1][0], Gr[1][6], acc[6]); SB(); \
    acc[7] = mfma16(Fr[0][3], Gr[0][4], acc[7]); SB(); \
    acc[5] = mfma16(Fr[1][1], Gr[1][4], acc[5]); SB(); \
    acc[6] = mfma16(Fr[1][1], Gr[1][5], acc[6]); SB(); \
    acc[7] = mfma16(Fr[1][0], Gr[1][7], acc[7]); SB(); \
    acc[6] = mfma16(Fr[1][2], Gr[1][4], acc[6]); SB(); \
    acc[7] = mfma16(Fr[1][1], Gr[1][6], acc[7]); SB(); \
    acc[7] = mfma16(Fr[1][2], Gr[1][5], acc[7]); SB(); \
    acc[7] = mfma16(Fr[1][3], Gr[1][4], acc[7]); SB(); \
    do {} while (0)

template <int LY>
DI void layer_body(LAS unsigned char* sm, const __amdgpu_buffer_rsrc_t rs, unsigned char* ws, u16* out, int tid, const int w, const int bid, const int rp, const int cp,
                   const int pj, const unsigned r_dsk, const unsigned r_b1, const unsigned tag, v4u (&W)[32], Dbg& dbg) {
  int lane;
  OPQ();
  const int w1b = WS_W1F + (cp * 7 + w) * 32768, w2b = WS_W2F + (cp * 112 + 14 * w) * 2048;
  const int hbase = (LY ? WS_H2 : WS_H1) + rp * (2 * 112 * 1024);
  const int xbuf = LY ? WS_XT3 : WS_XT1;
  const bool isO = w < 4, isY = w >= 4 && w < 7;
#define C_LOAD(hf) \
    ca = __builtin_amdgcn_raw_buffer_load_b64(rs, ((tid & 255) >> 3) * 128 + (tid & 7) * 8, WS_XT2 + pj * 8192 + (hf) * 4096, SC1); \
    cb = __builtin_amdgcn_raw_buffer_load_b64(rs, ((tid & 255) >> 3) * 128 + (tid & 7) * 8 + 64, WS_XT2 + pj * 8192 + (hf) * 4096, SC1); \
    _Pragma("unroll") for (int i = 0; i < 8; ++i) st[i] = LD128((tid & 255) * 16, WS_STP + ((hf) * 8 + i) * 4096);
#define C_BAD() (pz(pz(pz(pz(pz(pz(pz(pz(umax3(umax3(ca.x, ca.y, cb.x), cb.y, 0u), st[0]), st[1]), st[2]), st[3]), st[4]), st[5]), st[6]), st[7]) == POISON)
#define C_STAGE(hf) \
    if (__builtin_expect(wany(C_BAD()), 0)) { \
      _Pragma("unroll 1") for (int sp = 0;; ++sp) { \
        COUNT(53); \
        __builtin_amdgcn_s_sleep(2); \
        CFENCE(); \
        C_LOAD(hf) \
        if (!wany(C_BAD()) || sp >= SPIN_CAP) break; \
      } \
    } \
    { \
      float r[4], nb[4]; \
      unsigned one0 = 0x00003f80u; \
      asm volatile("" : "+v"(one0)); \
      _Pragma("unroll") for (int i = 0; i < 4; ++i) { \
        float ms = 0.f, qs = 8e-5f; \
        _Pragma("unroll") for (int c = 0; c < 8; ++c) { ms = dot2(st[c][i], one0, ms); qs = dot2(st[c][i], st[c][i], qs); } \
        const float mean = ms * 0.125f; \
        r[i] = __builtin_amdgcn_rsqf(__builtin_fmaxf(__builtin_fmaf(-mean, mean, qs * 0.125f), 1e-5f)); \
        nb[i] = -mean * r[i]; \
      } \
      stage_conv(sm, (hf) * 2048 + (tid & 255) * 8, ca, cb, r, nb); \
    }
  v4u xv[8];
  int hp[6];
#pragma unroll
    for (int j = 0; j < 6; ++j) { hp[j] = hbase + (w < 7 ? 14 * ((cp + 1 + j) & 7) + 2 * w : 14 * ((cp + 7) & 7) + 2 + 2 * j) * 1024; asm volatile("" : "+s"(hp[j])); }
  STAMP(8 + 24 * LY);
  {
    const int ch = w & 1, rq = w >> 1, b = lane & 15, g = lane >> 4;
    LAS unsigned char* fp = sm + LD_K + (ch * 4 + (b & 3)) * KCS + 4096 + 16 * g - 2 * (b & ~3) - 3584 - 128 * rq - 64;
    LAS unsigned char* gp = sm + LD_U + ch * 4608 + 480 + 32 * b + 16 * g - 128 * rq - 64;
    LAS unsigned char* exw = sm + LD_XT + ch * 16384 + rq * 1024 + (g * 16 + (b ^ (4 * g))) * 16;
    const int fch = (lane >> 3) & 1, fb = 8 * (w & 1) + 2 * (lane >> 4) + ((lane >> 2) & 1), fg = lane & 3;
    LAS unsigned char* exr = sm + LD_XT + fch * 16384 + (w >> 1) * 4096 + (fg * 16 + (fb ^ (4 * fg))) * 16;
    const int fpo = 256 * w + 64 * (lane >> 4) + 8 * (lane & 7);
    const float ds = fch ? bfhi(r_dsk) : bflo(r_dsk);
#define LDFH(r, p, hi) do { const v2u t_ = *(volatile LAS v2u*)(fp + 512 * (7 - (p)) + 64 - 64 * (r) + 8 * (hi)); if (hi) Fr[r][p] = v4u{fl_.x, fl_.y, t_.x, t_.y}; else fl_ = t_; } while (0)
#define LDG(r, q) Gr[r][q] = *(LAS v4u*)(gp + 512 * (q) + 64 - 64 * (r))
#define PZHOOK(k) do { if (LY == 0) { if (w < 7) ST128(PZ4, lane * 16 + ((k) & 1) * 1024, hbase + (((k) >> 1) * 112 + 14 * cp + 2 * w) * 1024); SB(); } } while (0)
#define CONV_FIN(hf) do { \
      const v2u uu = *(LAS v2u*)(sm + LD_U + fch * 4608 + 512 + (hf) * 2048 + fpo), xx = *(LAS v2u*)(sm + LD_X + fch * 4096 + (hf) * 2048 + fpo); \
      const f32x4 e0 = *(LAS f32x4*)(exr + (hf) * 32768), e1 = *(LAS f32x4*)(exr + (hf) * 32768 + 1024), e2 = *(LAS f32x4*)(exr + (hf) * 32768 + 2048), e3 = *(LAS f32x4*)(exr + (hf) * 32768 + 3072); \
      SB(); \
      float k0 = __builtin_fmaf(bflo(uu.x), ds, bflo(xx.x)), k1 = __builtin_fmaf(bfhi(uu.x), ds, bfhi(xx.x)), k2 = __builtin_fmaf(bflo(uu.y), ds, bflo(xx.y)), k3 = __builtin_fmaf(bfhi(uu.y), ds, bfhi(xx.y)); \
      asm("" : "+v"(k0)); asm("" : "+v"(k1)); asm("" : "+v"(k2)); asm("" : "+v"(k3)); \
      const f32x4 k = add4(f32x4{k0, k1, k2, k3}, add4(add4(e0, e1), add4(e2, e3))); \
      __builtin_amdgcn_raw_buffer_store_b64(v2u{pk(k[0], k[1]), pk(k[2], k[3])}, rs, w * 512 + lane * 8, xbuf + pj * 8192 + (hf) * 4096, SC1); \
      SB(); \
    } while (0)
    v4u Fr[2][8], Gr[2][8];
    f32x4 acc[8];
    v2u fl_, ca, cb; v4u st[8];
    const v4u PZ4 = {POISON, POISON, POISON, POISON};
#pragma unroll
    for (int i = 0; i < 8; ++i) acc[i] = f32x4{0.f, 0.f, 0.f, 0.f};
    __builtin_amdgcn_s_setprio(3);
    if (LY == 0) DRAIN();
    BAR();
    STAMP(9 + 24 * LY);
    CONV_S1();
#pragma unroll
    for (int c = 0; c < 4; ++c) *(LAS f32x4*)(exw + c * 4096) = acc[c];
    lds_add1(sm + C_EXA, lane);
    if (isO) __builtin_amdgcn_s_setprio(2);
    if (LY == 0 && bid == 0 && tid == 0) __builtin_amdgcn_raw_buffer_store_b32(tag, rs, 0, WS_EPOCH, SC1);
    if (!isO) { lds_wait_ge(sm + C_EXA, 8 * LY + 8); CONV_FIN(0); }
    {
      int wf = w;
      asm volatile("" : "+s"(wf));
      if (LY == 0) {
#pragma unroll
        for (int i = 0; i < 32; ++i) UNDEF(W[i]);
      }
      if (wf >= 4) __builtin_amdgcn_s_setprio(0);
      if (wf >= 4 && wf < 7) { LDW(YA(LY), YB(LY), w1b); }
      if (wf >= 4) __builtin_amdgcn_s_setprio(1);
    }
    CONV_S2A();
    if (isO) { lds_wait_ge(sm + C_EXA, 8 * LY + 8); CONV_FIN(0); }
    STAMP(26 + 24 * LY);
    if (LY) {
      UNDEF(ca); UNDEF(cb);
#pragma unroll
      for (int i = 0; i < 8; ++i) UNDEF(st[i]);
      if (isO) { C_LOAD(1) }
      SB();
    }
    CONV_S2B();
    STAMP(27 + 24 * LY);
    {
      int wf = w;
      asm volatile("" : "+s"(wf));
      if (LY) {
        if (wf < 4) { C_STAGE(1) }
        VM0();
      }
      asm volatile("" : "+s"(wf));
      if (LY == 0) { if (wf < 4) { __builtin_amdgcn_s_setprio(0); LDW(0, OM0, w1b); __builtin_amdgcn_s_setprio(2); } }
    }
    if (LY) BAR();
    STAMP(28 + 24 * LY);
    if (LY) {
      if (!isO) {
#pragma unroll
        for (int i = 0; i < 8; ++i) { xv[i] = LD128(((tid & 255) >> 3) * 8192 + (tid & 7) * 16, xbuf + rp * 128 + i * 262144); SB(); }
      }
    }
    CONV_S3();
#pragma unroll
    for (int c = 0; c < 4; ++c) *(LAS f32x4*)(exw + 32768 + c * 4096) = acc[4 + c];
    STAMP(10 + 24 * LY);
    if (LY) {
      if (isO) { __builtin_amdgcn_s_setprio(0); LDW(OA1, OS1, w1b); }
    }
    if (LY == 0) {
      if (isO) asm volatile("s_waitcnt vmcnt(%0)" :: "n"(OM0) : "memory"); else DRAIN();
      if (!isO) {
#pragma unroll
        for (int i = 0; i < 8; ++i) { xv[i] = LD128(((tid & 255) >> 3) * 8192 + (tid & 7) * 16, xbuf + rp * 128 + i * 262144); SB(); }
      }
    }
    __builtin_amdgcn_s_setprio(3);
    BAR();
    CONV_FIN(1);
  }
  STAMP(11 + 24 * LY);
  OPQ();
  const int q4 = (lane & 15) >> 2;
  LAS unsigned char* xp = sm + LD_XT + (8 * (lane >> 5) + q4) * 64 + ((lane >> 4) & 1) * 32 + 8 * (lane & 3);
  asm volatile("" : "+v"(xp));
  int wx = w;
  asm volatile("" : "+s"(wx));
  if (wx >= 4) {
    const int xo = ((tid & 255) >> 3) * 8192 + (tid & 7) * 16, xs = xbuf + rp * 128;
    float sa[8], qa[8];
    unsigned one2 = 0x3f803f80u;
    asm volatile("" : "+v"(one2));
#pragma unroll
    for (int e = 0; e < 8; ++e) { sa[e] = 0.f; qa[e] = 0.f; }
#pragma unroll
    for (int i = 0; i < 8; i += 2) {
      if (__builtin_expect(wany(pz(pz(0u, xv[i]), xv[i + 1]) == POISON), 0)) {
#pragma unroll 1
        for (int sp = 0;; ++sp) {
          COUNT(58);
          __builtin_amdgcn_s_sleep(2);
          CFENCE();
#pragma unroll
          for (int k = i; k < 8; ++k) { xv[k] = LD128(xo, xs + k * 262144); SB(); }
          if (!wany(pz(pz(0u, xv[i]), xv[i + 1]) == POISON) || sp >= SPIN_CAP) break;
        }
      }
      *(LAS v4u*)(sm + LD_XT + (tid & 255) * 16 + i * 4096) = xv[i];
      *(LAS v4u*)(sm + LD_XT + (tid & 255) * 16 + i * 4096 + 4096) = xv[i + 1];
#pragma unroll
      for (int e = 0; e < 4; ++e) {
        const unsigned lo = __builtin_amdgcn_perm(xv[i + 1][e], xv[i][e], 0x05040100u), hi = __builtin_amdgcn_perm(xv[i + 1][e], xv[i][e], 0x07060302u);
        sa[2 * e] = dot2(lo, one2, sa[2 * e]); qa[2 * e] = dot2(lo, lo, qa[2 * e]);
        sa[2 * e + 1] = dot2(hi, one2, sa[2 * e + 1]); qa[2 * e + 1] = dot2(hi, hi, qa[2 * e + 1]);
      }
      SB();
    }
    float t[8], u4[4];
#pragma unroll
    for (int i = 0; i < 8; ++i) t[i] = swap32add(sa[i], qa[i]);
#pragma unroll
    for (int i = 0; i < 4; ++i) u4[i] = dppadd<0x124>(dppadd<0x128>(swap16add(t[i], t[i + 4])));
    *(LAS f32x4*)(sm + LD_P2 + (w & 3) * 256 + ((lane >> 5) * 32 + 8 * (lane & 3) + 4 * ((lane >> 4) & 1)) * 4) = f32x4{u4[0], u4[1], u4[2], u4[3]};
  }
  __builtin_amdgcn_s_waitcnt(0x0F71);
  if (isO) { __builtin_amdgcn_s_setprio(0); LDW(LY ? OS1 : OM0, 32, w1b); }
  STAMP(13 + 24 * LY);
  __builtin_amdgcn_s_setprio(3);
  BAR();
  STAMP(14 + 24 * LY);
  v4u Ho[4], Ha[12], E[NE];
#define W2(j) ((j) < NE ? E[(j) < NE ? (j) : 0] : W[(j) < NE ? 0 : (j) - NE])
#define ROWC(sum, sq, hf) do { \
      float m_, sd_, mean_ = (sum) * (1.0f / D_); \
      asm("" : "+v"(mean_)); \
      ln_consts(mean_, (sq) * (1.0f / D_), m_, sd_); \
      if (lane < 32) *(LAS v4u*)(sm + LD_P1 + (hf) * 512 + lane * 16) = v4u{pk(-m_, -m_), pk(-m_, sd_), f2u(1.44269504088896f * __builtin_amdgcn_rcpf(sd_)), 0u}; \
      LGKM0(); \
      if (lane == 0) *(volatile LAS unsigned*)(sm + ((hf) ? F_STB : F_STA)) = LY + 1; \
    } while (0)
  if (!isO) {
    const int xo = ((tid & 255) >> 3) * 8192 + (tid & 7) * 16, xs = xbuf + 4096 + rp * 128;
    if (DLY_X) __builtin_amdgcn_s_sleep(DLY_X);
#pragma unroll
    for (int i = 0; i < 8; ++i) { xv[i] = LD128(xo, xs + i * 262144); SB(); }
    if (w == 7) {
      float tt = *(LAS float*)(sm + LD_P2 + lane * 4);
#pragma unroll
      for (int i = 1; i < 4; ++i) tt = fadd(tt, *(LAS float*)(sm + LD_P2 + i * 256 + lane * 4));
      const auto mq = __builtin_amdgcn_permlane32_swap(f2u(tt), f2u(tt), false, false);
      ROWC(u2f(mq[0]), u2f(mq[1]), 0);
    }
#pragma unroll
    for (int i = 0; i < 8; i += 2) {
      if (__builtin_expect(wany(pz(pz(0u, xv[i]), xv[i + 1]) == POISON), 0)) {
#pragma unroll 1
        for (int sp = 0;; ++sp) {
          COUNT(57);
          __builtin_amdgcn_s_sleep(2);
          CFENCE();
#pragma unroll
          for (int k = i; k < 8; ++k) { xv[k] = LD128(xo, xs + k * 262144); SB(); }
          if (!wany(pz(pz(0u, xv[i]), xv[i + 1]) == POISON) || sp >= SPIN_CAP) break;
        }
      }
      *(LAS v4u*)(sm + LD_XT + 32768 + (tid & 255) * 16 + i * 4096) = xv[i];
      *(LAS v4u*)(sm + LD_XT + 32768 + (tid & 255) * 16 + i * 4096 + 4096) = xv[i + 1];
      SB();
    }
    lds_add1(sm + C_XB, lane);
    STAMP(15 + 24 * LY);
  }
  int w7 = w;
  asm volatile("" : "+s"(w7));
  if (w7 == 7) {
    lds_wait_ge(sm + C_XB, 4 * LY + 4);
    {
      float s0 = 0.f, q0 = 0.f, s1 = 0.f, q1 = 0.f;
      unsigned one2 = 0x3f803f80u;
      asm volatile("" : "+v"(one2));
      __builtin_amdgcn_s_setprio(3);
      v4u xf[2][8];
#pragma unroll
      for (int ks = 0; ks < 8; ++ks) xf[0][ks] = trfrag(xp + 32768 + ks * 1024, 256);
#pragma unroll
      for (int it = 0; it < 4; ++it) {
        asm volatile("" : "+v"(xf[it & 1][0]), "+v"(xf[it & 1][1]), "+v"(xf[it & 1][2]), "+v"(xf[it & 1][3]), "+v"(xf[it & 1][4]), "+v"(xf[it & 1][5]), "+v"(xf[it & 1][6]), "+v"(xf[it & 1][7]));
        if (it < 3) {
#pragma unroll
          for (int ks = 0; ks < 8; ++ks) xf[(it + 1) & 1][ks] = trfrag(xp + 32768 + (it + 1) * 8192 + ks * 1024, 256);
        }
        SB();
#pragma unroll
        for (int ks = 0; ks < 8; ++ks) {
          const v4u f = xf[it & 1][ks];
          s0 = dot2(f.x, one2, s0); q0 = dot2(f.x, f.x, q0); s1 = dot2(f.y, one2, s1); q1 = dot2(f.y, f.y, q1);
          s0 = dot2(f.z, one2, s0); q0 = dot2(f.z, f.z, q0); s1 = dot2(f.w, one2, s1); q1 = dot2(f.w, f.w, q1);
        }
        asm volatile("" : "+v"(s0), "+v"(q0), "+v"(s1), "+v"(q1));
        SB();
      }
      const float st_ = fadd(s0, s1), qt_ = fadd(q0, q1);
      ROWC(swap32add(st_, st_), swap32add(qt_, qt_), 1);
    }
    STAMP(29 + 24 * LY - 22 * LY);
#pragma unroll 2
    for (int i = 0; i < 8; ++i) {
      if (LY == 0) {
        *(LAS v4u*)(sm + LD_EX + i * 1024 + lane * 16) = *(LAS v4u*)(sm + LD_XT + (i >> 2) * 32768 + cp * 4096 + (i & 3) * 1024 + lane * 16);
      } else {
        const int lq = i & 3, dc = (i >> 2) * 4 + (lane >> 4);
        const v4u t = trfrag(sm + LD_XT + (lq >> 1) * 32768 + (64 * cp + 8 * dc + q4) * 64 + (lq & 1) * 32 + 8 * (lane & 3), 256);
        *(LAS v4u*)(sm + LD_EX + (16 * lq + (lane & 15)) * 128 + dc * 16) = t;
      }
    }
    lds_add1(sm + C_XDONE, lane);
    __builtin_amdgcn_s_setprio(0);
    if (LY == 0) {
#pragma unroll
      for (int j = 0; j < 28; ++j) { W[(j + 28 - NE) % 28] = LD128(lane * 16 + (j & 3) * 1024, w2b + (j >> 2) * 4096); SB(); }
    }
#pragma unroll
    for (int i = 0; i < NE; ++i) { E[i] = W[28 - NE + i]; asm volatile("" : "+v"(E[i])); UNDEF(W[28 - NE + i]); }
#define W7PAIR(hf) _Pragma("unroll 1") for (int sp = 0;; ++sp) { \
      CFENCE(); \
      Ho[2 * (hf)] = LD128(lane * 16, hp[0] - 2048 + (hf) * (112 * 1024)); Ho[2 * (hf) + 1] = LD128(lane * 16 + 1024, hp[0] - 2048 + (hf) * (112 * 1024)); \
      if (!wany(pz(pz(0u, Ho[2 * (hf)]), Ho[2 * (hf) + 1]) == POISON) || sp >= SPIN_CAP) break; \
      __builtin_amdgcn_s_sleep(4); \
    }
    __builtin_amdgcn_s_setprio(2);
    W7PAIR(0)
  }
  if (w < 7) {
    if (YB(LY) < 32) { if (isY) { __builtin_amdgcn_s_setprio(0); LDW(YB(LY), 32, w1b); __builtin_amdgcn_s_setprio(3); } }
    v2u cf;
    if (LY) cf = *(LAS v2u*)(sm + LD_CB + w * 512 + lane * 8);
#pragma unroll
    for (int hf = 0; hf < 2; ++hf) {
      if (hf) {
        lds_wait_ge(sm + (w == 3 ? F_STB : C_XB), w == 3 ? LY + 1 : 4 * LY + 4);
      }
      f32x16 acc = zero16();
      f32x4 cs = {0.f, 0.f, 0.f, 0.f};
      const unsigned sel = (((lane >> 2) ^ (lane >> 4)) & 1) ? 0u : 0x3f803f80u;
      constexpr int PD = 5;
      v4u xb[PD], sf; unsigned fv;
#pragma unroll
      for (int i = 0; i < PD; ++i) xb[i] = trfrag(xp + hf * 32768 + i * 1024, 256);
      SB();
#pragma unroll
      for (int ks = 0; ks < 32; ++ks) {
        acc = mfma32(W[ks], xb[ks % PD], acc);
        if (LY == 0 && hf == 0) cs = mfma16(v4u{sel, sel, sel, sel}, W[ks], cs);
        SB();
        if (ks + PD < 32) xb[ks % PD] = trfrag(xp + hf * 32768 + (ks + PD) * 1024, 256);
        if (hf == 0 && (ks & 1) && (ks >> 1) < NE) E[ks >> 1] = LD128(lane * 16 + ((ks >> 1) & 3) * 1024, w2b + (ks >> 3) * 4096);
        if (hf == 1 && (ks & 1) && (ks >> 1) + NE < 28) W[ks >> 1] = LD128(lane * 16 + (((ks >> 1) + NE) & 3) * 1024, w2b + (((ks >> 1) + NE) >> 2) * 4096);
        if (ks == 27) {
          fv = *(volatile LAS unsigned*)(sm + (hf ? F_STB : F_STA));
          CFENCE();
          sf = *(LAS v4u*)(sm + LD_P1 + hf * 512 + (lane & 31) * 16);
        }
        SB();
      }
      static_assert(NE >= 12, "the remaining weights are requested in 16 steps");
      if (LY == 0 && hf == 0) {
        const float c0 = rbf(cs[0]), c1 = rbf(fadd(cs[0], -c0)), c2 = fadd(fadd(cs[0], -c0), -c1);
        cf = lane < 32 ? v2u{pk(c0, c1), pk(c2, bflo(r_b1))} : v2u{0u, 0u};
        *(LAS v2u*)(sm + LD_CB + w * 512 + lane * 8) = cf;
      }
      if (wany(fv < LY + 1)) {
        lds_wait_ge(sm + (hf ? F_STB : F_STA), LY + 1);
        sf = *(LAS v4u*)(sm + LD_P1 + hf * 512 + (lane & 31) * 16);
      }
      acc = mfma32(v4u{cf.x, cf.y, 0u, 0u}, v4u{sf.x, sf.y, 0u, 0u}, acc);
      if (hf) lds_add1(sm + C_XDONE, lane);
      else __builtin_amdgcn_s_setprio(2);
      STAMP(16 + 24 * LY + 14 * hf - 14 * hf * LY - 8 * hf * LY + 16 * hf * LY);
      const float r = u2f(sf.z);
#pragma unroll
      for (int s = 0; s < 2; ++s) {
        float h[8], ex[8], d[8], o8[8];
#pragma unroll
        for (int e = 0; e < 8; ++e) h[e] = acc[8 * s + e] * r;
#pragma unroll
        for (int e = 0; e < 8; ++e) ex[e] = __builtin_amdgcn_exp2f(h[e]);
#pragma unroll
        for (int e = 0; e < 8; ++e) d[e] = __builtin_amdgcn_rcpf(__builtin_fmaf(ex[e], ex[e] + 2.f, 2.f));
#pragma unroll
        for (int e = 0; e < 8; ++e) o8[e] = h[e] * __builtin_fmaf(d[e], -1.38629436111989f, 0.693147180559945f);
        asm("" : "+v"(o8[0]), "+v"(o8[1]), "+v"(o8[2]), "+v"(o8[3]), "+v"(o8[4]), "+v"(o8[5]), "+v"(o8[6]), "+v"(o8[7]));
        Ho[2 * hf + s] = v4u{pk(o8[0], o8[1]), pk(o8[2], o8[3]), pk(o8[4], o8[5]), pk(o8[6], o8[7])};
        ST128(Ho[2 * hf + s], lane * 16 + s * 1024, hbase + (hf * 112 + 14 * cp + 2 * w) * 1024);
        SB();
      }
    }
    lds_add1(sm + (isY ? C_PUBY : C_PUBO), lane);
  }
  STAMP(17 + 24 * LY);
  OPQ();
#define MM(h, wv, c) (LY ? mfma32((wv), (h), (c)) : mfma32((h), (wv), (c)))
#define SKEY(dt, g) (LY ? 32 * ((dt) * 4 + (g)) : 64 * (g))
#define SADDR(dt, g) (slab + (dt) * 4096 + (g) * 1024 + (lx ^ SKEY(dt, g)))
#define PIN2(x, y) do { asm volatile("" : "+v"(x), "+v"(y)); SB(); } while (0)
#define CHK(h0, h1) __builtin_amdgcn_ballot_w64(pz(pz(0u, h0), h1) == POISON)
#define PAIR(lt, j, h0, h1, a0, a1, n0, n1) \
    if (__builtin_expect(bad != 0, 0)) { \
      _Pragma("unroll 1") for (int sp = 0;; ++sp) { \
        COUNT(56); \
        __builtin_amdgcn_s_sleep(HSLP); \
        CFENCE(); \
        h0 = LD128(lane * 16, hp[j] + (lt) * (112 * 1024)); h1 = LD128(lane * 16 + 1024, hp[j] + (lt) * (112 * 1024)); \
        if (!CHK(h0, h1) || sp >= SPIN_CAP) break; \
      } \
    } \
    a0 = MM(h0, W2(4 * (j) + 4), a0); a1 = MM(h0, W2(4 * (j) + 5), a1); \
    a0 = MM(h1, W2(4 * (j) + 6), a0); \
    PIN2(a0, a1); \
    bad = CHK(n0, n1); \
    asm volatile("" : "+s"(bad)); \
    SB(); \
    a1 = MM(h1, W2(4 * (j) + 7), a1); \
    PIN2(a0, a1);
#define PUT(dt, g, a) *(LAS f32x4*)SADDR(dt, g) = f32x4{a[4 * (g)], a[4 * (g) + 1], a[4 * (g) + 2], a[4 * (g) + 3]}
#define PUTA() _Pragma("unroll") for (int g = 0; g < 4; ++g) PUT(0, g, a0); _Pragma("unroll") for (int g = 0; g < 4; ++g) PUT(1, g, a1);
#define LDH(H, lo, hi, j0, lt) _Pragma("unroll") for (int i = (lo); i < (hi); ++i) { H[i] = LD128(lane * 16 + (i & 1) * 1024, hp[(j0) + ((i - (lo)) >> 1)] + (lt) * (112 * 1024)); SB(); }
#define FINISH(hf, FIRST) do { \
    const int dt = LY ? (lane >> 2) & 1 : (w >> 1) & 1, g = lane & 3, r = LY ? (w & 3) * 8 + (lane >> 3) : (w & 1) * 16 + (lane >> 2); \
    LAS unsigned char* sp = sm + ((LY && (hf)) ? 0 : LD_XT) + dt * 4096 + g * 1024 + ((r << 4) ^ SKEY(dt, g)); \
    const v4u xres = *(LAS v4u*)(sm + LD_EX + (hf) * 4096 + (w & 3) * 1024 + lane * 16); \
    f32x4 lo = {bflo(xres.x), bfhi(xres.x), bflo(xres.y), bfhi(xres.y)}, hi = {bflo(xres.z), bfhi(xres.z), bflo(xres.w), bfhi(xres.w)}; \
    _Pragma("unroll") for (int v_ = 0; v_ < 2; ++v_) { \
      const int v = 4 * (v_ ^ (FIRST)); \
      lds_wait_ge(sm + ((hf) ? C_SLB : C_SLA) + (v ? 64 : 0), 4 * LY + 4); \
      f32x4 pl[4], ph[4]; \
      _Pragma("unroll") for (int k = 0; k < 4; ++k) { pl[k] = *(LAS f32x4*)(sp + (v + k) * 8192); ph[k] = *(LAS f32x4*)(sp + (v + k) * 8192 + 512); } \
      if (v_ && (hf) == 0 && LY == 0) lds_add1(sm + C_FINA, lane); \
      SB(); \
      if ((hf) == 0 && LY == 0) { lo = add4(lo, add4(add4(pl[0], pl[1]), add4(pl[2], pl[3]))); hi = add4(hi, add4(add4(ph[0], ph[1]), add4(ph[2], ph[3]))); } \
      else { lo += (pl[0] + pl[1]) + (pl[2] + pl[3]); hi += (ph[0] + ph[1]) + (ph[2] + ph[3]); } \
      asm volatile("" : "+v"(lo), "+v"(hi)); \
    } \
    const v4u o = {pk(lo[0], lo[1]), pk(lo[2], lo[3]), pk(hi[0], hi[1]), pk(hi[2], hi[3])}; \
    if (LY == 1) { \
      *(v4u*)(out + (1024 * (hf) + 32 * rp + r) * D_ + 64 * cp + 8 * (lane & 7)) = o; \
    } else { \
      ST128(o, ((w & 3) * 8 + (lane >> 3)) * 8192 + (lane & 7) * 16, WS_XT2 + 32 * cp * 8192 + (hf) * 4096 + rp * 128); \
      float v[8], t[8], u4[4]; \
      _Pragma("unroll") for (int i = 0; i < 4; ++i) { v[2 * i] = bflo(o[i]); v[2 * i + 1] = bfhi(o[i]); } \
      _Pragma("unroll") for (int i = 0; i < 8; ++i) { float sq_ = v[i] * v[i]; asm("" : "+v"(sq_)); t[i] = swap32add(v[i], sq_); asm("" : "+v"(t[i])); } \
      _Pragma("unroll") for (int i = 0; i < 4; ++i) { u4[i] = swap16add(t[i], t[i + 4]); asm("" : "+v"(u4[i])); u4[i] = dppadd<0x128>(u4[i]); asm("" : "+v"(u4[i])); u4[i] = dppadd<0x124>(u4[i]); asm("" : "+v"(u4[i])); } \
      if ((lane & 12) == 0) *(LAS f32x4*)(sm + LD_P2 + 2048 + (hf) * 1024 + (w & 3) * 256 + ((lane >> 5) * 32 + 8 * g + 4 * ((lane >> 4) & 1)) * 4) = f32x4{u4[0], u4[1], u4[2], u4[3]}; \
      CFENCE(); \
      unsigned old = 0; \
      if (lane == 0) old = __hip_atomic_fetch_add((LAS unsigned*)(sm + C_ST + 4 * (hf)), 1u, __ATOMIC_RELAXED, __HIP_MEMORY_SCOPE_WORKGROUP); \
      if (__builtin_amdgcn_readfirstlane(old) == 3) {     \
        CFENCE(); \
        float a[4]; \
        _Pragma("unroll") for (int i = 0; i < 4; ++i) a[i] = *(LAS float*)(sm + LD_P2 + 2048 + (hf) * 1024 + i * 256 + lane * 4); \
        const unsigned tt = f2u(fadd(fadd(a[0], a[1]), fadd(a[2], a[3])) * (1.0f / 64.f)); \
        const auto mq = __builtin_amdgcn_permlane32_swap(tt, tt, false, false);     \
        const float mp = u2f(mq[0]), dv = __builtin_amdgcn_sqrtf(__builtin_fmaxf(__builtin_fmaf(-mp, mp, u2f(mq[1])), 0.f)); \
        if (lane < 32) __builtin_amdgcn_raw_buffer_store_b32(pk(mp, dv), rs, lane * 4, WS_STP + ((hf) * 8 + cp) * 4096 + rp * 128, SC1); \
      } \
    } \
  } while (0)
  {
    LAS unsigned char* slab = sm + LD_XT + w * 8192;
    const int lx = lane << 4;
    u64 bad;
    f32x16 a0, a1;
    v4u Hc[NHC];
    lds_wait_ge(sm + C_XDONE, 8 * LY + 8);
    __builtin_amdgcn_s_setprio(1);
    LDH(Ha, 0, 12, 0, 0)
    a0 = MM(Ho[0], W2(0), zero16()); a1 = MM(Ho[0], W2(1), zero16());
    a0 = MM(Ho[1], W2(2), a0); a1 = MM(Ho[1], W2(3), a1);
    PIN2(a0, a1);
    bad = CHK(Ha[0], Ha[1]);
    STAMP(18 + 24 * LY);
#pragma unroll
    for (int j = 0; j < 6; ++j) {
      PAIR(0, j, Ha[2 * j], Ha[2 * j + 1], a0, a1, Ha[j < 5 ? 2 * j + 2 : 10], Ha[j < 5 ? 2 * j + 3 : 11])
      if (j == 0) { if (w == 7) lds_wait_ge(sm + C_PUBY, 3 * LY + 3); LDH(Hc, 0, NHC, 0, 1) }
      if (j + NHC / 2 < 6) { LDH(Ha, 2 * j, 2 * j + 2, j + NHC / 2, 1) }
    }
    STAMP(19 + 24 * LY);
    PUTA()
    lds_add1(sm + C_SLA + (isO ? 0 : 64), lane);
    if (w == 7) { W7PAIR(1) }
    __builtin_amdgcn_s_setprio(0);
    if (LY == 0) { if (isO) { __builtin_amdgcn_s_setprio(3); FINISH(0, 0); __builtin_amdgcn_s_setprio(0); } }
    STAMP(22 + 24 * LY);
    a0 = MM(Ho[2], W2(0), zero16()); a1 = MM(Ho[2], W2(1), zero16());
    a0 = MM(Ho[3], W2(2), a0); a1 = MM(Ho[3], W2(3), a1);
    PIN2(a0, a1);
    bad = CHK(Hc[0], Hc[1]);
#define H1(i) ((i) < NHC ? Hc[(i) < NHC ? (i) : 0] : Ha[(i) < NHC ? 0 : (i) - NHC])
#pragma unroll
    for (int j = 0; j < 6; ++j) {
      PAIR(1, j, H1(2 * j), H1(2 * j + 1), a0, a1, H1(j < 5 ? 2 * j + 2 : 10), H1(j < 5 ? 2 * j + 3 : 11))
    }
    STAMP(20 + 24 * LY);
    if (LY) slab -= LD_XT; else lds_wait_ge(sm + C_FINA, 4);
    PUTA()
    lds_add1(sm + C_SLB + (isO ? 0 : 64), lane);
  }
  STAMP(21 + 24 * LY);
  OPQ();
  __builtin_amdgcn_s_setprio(3);
  if (LY == 0) { if (isO) { FINISH(1, 1); } }
  else { const int hfw = w >> 2; FINISH(hfw, 0); }
  __builtin_amdgcn_s_setprio(0);
  STAMP(23 + 24 * LY);
  if (LY == 0) {
#pragma unroll
    for (int i = 28; i < 32; ++i) UNDEF(W[i]);
#pragma unroll
    for (int i = 0; i < NE; ++i) W[28 - NE + i] = E[i];
    int wf = w;
    asm volatile("" : "+s"(wf));
    if (wf >= 4) {
      v2u ca, cb; v4u st[8];
#pragma unroll
      for (int p = 0; p < 7; ++p) {
        const int f = (w - 4) * 7 + p;
        ST128((v4u{POISON, POISON, POISON, POISON}), lane * 16, WS_H2 + rp * (2 * 112 * 1024) + cp * 14336 + (f + (f < 14 ? 0 : 98)) * 1024);
      }
      __builtin_amdgcn_s_sleep(DLY_C);
      __builtin_amdgcn_s_setprio(1);
      C_LOAD(0)
      C_STAGE(0)
      DRAIN();
      __builtin_amdgcn_s_setprio(0);
    }
    STAMP(25);
    if (isO) { LDW(0, OA(1), w1b); }
    if (isY) { LDW(0, YA(1), w1b); }
  }
}

__global__ void __launch_bounds__(512, 2)
fused_fwd(const u16* __restrict__ x0, const u16* __restrict__ lar, const u16* __restrict__ aim, const u16* __restrict__ crp,
          const u16* __restrict__ cip, const u16* __restrict__ ldt, const u16* __restrict__ dsk, const u16* __restrict__ w1,
          const u16* __restrict__ b1, const u16* __restrict__ w2, u16* __restrict__ out, unsigned char* ws) {
  extern __shared__ __attribute__((aligned(16))) unsigned char smem_[];
  LAS unsigned char* sm = (LAS unsigned char*)smem_;
  const int tid = threadIdx.x, lane = tid & 63;
  const int w = __builtin_amdgcn_readfirstlane(tid >> 6);
  const int bid = blockIdx.x, xcd = bid & 7, slot = bid >> 3;
  const int rp = (xcd >> (3 - MAPB)) * (32 >> MAPB) + (slot >> MAPB), cp = ((xcd & ((1 << (3 - MAPB)) - 1)) << MAPB) + (slot & ((1 << MAPB) - 1)), pj = xcd * 32 + slot;
  const __amdgpu_buffer_rsrc_t rs = __builtin_amdgcn_make_buffer_rsrc((void*)ws, 0, WS_END, 0x00020000);
  Dbg dbg;
  STAMP(0);
  typedef const __attribute__((address_space(4))) unsigned* cptr;
  const u64 pc_ = (__builtin_amdgcn_s_getpc() & ~127ull) + imin((slot * 8 + w) * 192, SPF_BYTES);
  const unsigned cpf0 = *(cptr)pc_, cpf1 = *(cptr)(pc_ + 64), cpf2 = *(cptr)(pc_ + 128);
  SB();
  ST128((v4u{POISON, POISON, POISON, POISON}), tid * 16, WS_XT1 + bid * 8192);
  SB();
  const int kch = w >> 2, role = ((w >> 1) ^ (w >> 2)) & 1, mode_n = (w & 1) * 64 + lane, dk = 2 * pj + kch;
  const unsigned epoch = __builtin_amdgcn_raw_buffer_load_b32(rs, WS_EPOCH, 0, SC1);
  const v4u xr = *(const v4u*)(x0 + (bid * 8 + w) * D_ + lane * 8);
  const v4u xc = *(const v4u*)(x0 + (64 * rp + (tid >> 3)) * D_ + 64 * cp + 8 * (tid & 7));
  const int wq = w < 7 ? w : 6;
  const int ksg = (bid * 7 + wq) >> 4, dtile = (bid * 7 + wq) & 15;
  const v4u g1 = *(const v4u*)(w1 + (16 * (bid >> 3) + (lane >> 2)) * F_ + 224 * (bid & 7) + 32 * wq + 8 * (lane & 3));
  const unsigned r_lar = lar[dk * N_ + mode_n], r_aim = aim[dk * N_ + mode_n], r_cr = crp[dk * N_ + mode_n], r_ci = cip[dk * N_ + mode_n];
  const unsigned r_ldt = ldt[dk];
  SB();
  const v4u g2 = *(const v4u*)(w2 + (16 * ksg + (lane >> 2)) * D_ + 32 * dtile + 8 * (lane & 3));
  const unsigned r_dsk = *(const unsigned*)(dsk + 2 * pj);
  const unsigned r_b1 = b1[224 * cp + 32 * wq + (lane & 31)];
  SB();
  SB();
  if (tid < 32) *(LAS unsigned*)(sm + LD_FL + tid * 4) = 0u;
  int kidx[11], sidx[4];
  {
    expand_addr(tid, kidx);
    const int rt = (w >> 1) & 1, g = lane >> 4, j = 16 * (w & 1) + (lane & 15);
    const int b0 = rt ? 512 + 128 * g + j : g < 2 ? 4 * g + 8 * j : 128 * g + (j << (4 - g));
    const int d1 = rt ? 32 : 1, d2 = (rt || g == 3) ? 64 : 2;
    sidx[0] = LD_ST + kch * STB + 2 * b0; sidx[1] = sidx[0] + 2 * d1; sidx[2] = sidx[0] + 2 * d2; sidx[3] = sidx[2] + 2 * d1;
#pragma unroll
    for (int m = 0; m < 11; ++m) asm volatile("" : "+v"(kidx[m]));
#pragma unroll
    for (int m = 0; m < 4; ++m) asm volatile("" : "+v"(sidx[m]));
  }
  asm volatile("" :: "s"(cpf0), "s"(cpf1), "s"(cpf2));
  SB();
  STAMP(1);
  const unsigned tag = epoch * 0x9E3779B1u + 0x7F4A7C15u;
  {
    float s = 0.f, q = 0.f;
#pragma unroll
    for (int i = 0; i < 4; ++i) { s = dot2(xr[i], 0x3f803f80u, s); q = dot2(xr[i], xr[i], q); }
    float t = swap32add(s, q);
    t = dppadd<0xB1>(t); t = dppadd<0x4E>(t); t = dppadd<0x141>(t); t = dppadd<0x140>(t);
    t = swap16add(t, t);
    const float sum = u2f(__builtin_amdgcn_readlane(f2u(t), 0));
    const float sq = u2f(__builtin_amdgcn_readlane(f2u(t), 32));
    float m, sd; ln_consts(sum * (1.0f / D_), sq * (1.0f / D_), m, sd);
    const unsigned gran = (f2u(m) >> 16) | (f2u(sd) & 0xffff0000u);
    asm volatile("" :: "v"(gran));
      *(LAS v4u*)(sm + LD_K + (tid >> 3) * 144 + (tid & 7) * 16) = xc;
    asm volatile("s_waitcnt vmcnt(8)" ::: "memory");
    if (lane == 0) *(LAS unsigned*)(sm + LD_P1 + w * 4) = gran;
  }
  BAR();
  {
    LAS unsigned char* tp = sm + LD_K + (tid & 15) * 288 + (tid >> 4) * 4;
    const unsigned a0 = *(LAS unsigned*)tp, a1 = *(LAS unsigned*)(tp + 144), a2 = *(LAS unsigned*)(tp + 4608), a3 = *(LAS unsigned*)(tp + 4752);
    if (tid < 4) {
      const v2u g2_ = *(LAS v2u*)(sm + LD_P1 + tid * 8);
      ST128((v4u{g2_.x, tag, g2_.y, tag}), tid * 16, WS_ST0 + bid * 64);
    }
    const int xo = (tid >> 4) * 16384 + (tid & 15) * 16;
    ST128((v4u{a0, tag, a1, tag}), xo, WS_XG + cp * (32 * 16384) + rp * 512);
    ST128((v4u{a2, tag, a3, tag}), xo + 256, WS_XG + cp * (32 * 16384) + rp * 512);
  }
  {
    LAS unsigned char* wt = sm + LD_WT + wq * 2048;
    *(LAS v4u*)(wt + lane * 16) = g1;
    const v4u a = trfrag(wt + (8 * (lane >> 5) + ((lane & 15) >> 2)) * 64 + ((lane >> 4) & 1) * 32 + 8 * (lane & 3), 256);
    ST128(a, lane * 16, WS_W1F + (((bid & 7) * 7 + wq) * 32 + (bid >> 3)) * 1024);
  }
  STAMP(2);
  {
    const float dt = rbf(fexp(bflo(r_ldt)));
    const float ar = -rbf(fexp(bflo(r_lar))) * dt, ai = bflo(r_aim) * dt;
    const float ar2 = ar * 1.44269504088896f, rev = ai * 0.159154943091895f;
    LAS unsigned char* tb = sm + LD_XT + (kch * 2 + role) * (32 * TROW) + mode_n * 4;
#define PUTT(row, v) do { const f32x2 v_ = (v); *(LAS unsigned*)(tb + (row) * TROW) = pk(v_.x, v_.y); } while (0)
    if (role == 0) {
      const f32x2 e1 = cexp_(ar2, -rev, 1.f), e2 = cexp_(ar2, -rev, 256.f);
      const f32x2 p2 = cmul(e1, e1), p3 = cmul(p2, e1), p4 = cmul(p2, p2), p5 = cmul(p4, e1), p6 = cmul(p4, p2), p7 = cmul(p3, p4);
      const f32x2 W2 = cmul(e2, e2), W3 = cmul(W2, e2), W4 = cmul(W2, W2);
      *(LAS unsigned*)(tb) = 0x00003f80u;
      PUTT(1, e1); PUTT(2, p2); PUTT(3, p3); PUTT(4, p4); PUTT(5, p5); PUTT(6, p6); PUTT(7, p7);
      PUTT(8, e2); PUTT(9, cmul(p2, e2)); PUTT(10, cmul(p4, e2)); PUTT(11, cmul(p6, e2));
      PUTT(12, W2); PUTT(13, cmul(W2, p4)); PUTT(14, W3); PUTT(15, cmul(W3, p4));
      PUTT(16, W4); PUTT(17, cmul(W4, e2)); PUTT(18, cmul(W4, W2)); PUTT(19, cmul(W3, W4)); PUTT(20, cmul(W4, W4));
    } else {
      const f32x2 e1 = cexp_(ar2, rev, 8.f), e2 = f32x2{2.f * bflo(r_cr) * dt, 2.f * bflo(r_ci) * dt};
      const f32x2 q2 = cmul(e1, e1), q4 = cmul(q2, q2);
      f32x2 b0 = e2, b1_ = cmul(e2, e1), b2 = cmul(e2, q2), b3 = cmul(b1_, q2);
#pragma unroll 1
      for (int a = 0; a < 8; ++a) {
        PUTT(0, b0); PUTT(1, b1_); PUTT(2, b2); PUTT(3, b3);
        b0 = cmul(b0, q4); b1_ = cmul(b1_, q4); b2 = cmul(b2, q4); b3 = cmul(b3, q4);
        tb += 4 * TROW;
      }
    }
  }
  STAMP(3);
  asm volatile("" :: "v"(g2), "v"(r_b1));
  DRAIN();
  {
    LAS unsigned char* wt = sm + LD_WT + wq * 2048 + 1024;
    *(LAS v4u*)(wt + lane * 16) = g2;
    const v4u b = trfrag(wt + (4 * (lane >> 5) + ((lane & 15) >> 2)) * 64 + ((lane >> 4) & 1) * 32 + 8 * (lane & 3), 512);
    const int c2 = ksg / 14, rem = ksg - 14 * c2;
    const int vp = rem >> 1, jj = (c2 - (dtile >> 1)) & 7;
    const int sl = (jj < 7 ? 14 * vp + 2 * jj : 98 + 2 * vp) + (rem & 1);
    if (w < 7) ST128(b, lane * 16, WS_W2F + (((dtile >> 1) * 112 + sl) * 2 + (dtile & 1)) * 1024);
  }
  if (tid >= 496) ST128((v4u{POISON, POISON, POISON, POISON}), (tid & 15) * 16, WS_STP + bid * 256);
  if (role == 0) {
#pragma unroll
    for (int i = 0; i < 4; ++i) ST128((v4u{POISON, POISON, POISON, POISON}), (tid & 127) * 16 + i * 2048, (kch ? WS_XT3 : WS_XT2) + bid * 8192);
  }
  BAR();
  if (tid == 0) __builtin_amdgcn_raw_buffer_store_b32(tag, rs, 0, WS_WRDY + ((bid & 7) * 32 + (bid >> 3)) * 128, SC1);
  STAMP(4);
  {
    const int rt = (w >> 1) & 1, ct = w & 1, i16 = lane & 15, g = lane >> 4;
    LAS unsigned char* ta = sm + LD_XT + (kch * 2) * (32 * TROW) + (16 * rt + i16) * TROW + g * 16;
    LAS unsigned char* tbp = sm + LD_XT + (kch * 2 + 1) * (32 * TROW) + (16 * ct + i16) * TROW + g * 16;
    v4u fa[8], fb[8];
#pragma unroll
    for (int ks = 0; ks < 8; ++ks) { fa[ks] = *(LAS v4u*)(ta + ks * 64); fb[ks] = *(LAS v4u*)(tbp + ks * 64); }
    SB();
    f32x4 acc = {0.f, 0.f, 0.f, 0.f};
#pragma unroll
    for (int ks = 0; ks < 8; ++ks) acc = mfma16(fa[ks], fb[ks], acc);
#pragma unroll
    for (int reg = 0; reg < 4; ++reg) *(LAS u16*)(sm + sidx[reg]) = (u16)pk(acc[reg], acc[reg]);
  }
  const int sp8 = stage_pos(tid) * 8;
  v4u ga = LD128(sp8, WS_ST0), gb = LD128(sp8 + 16, WS_ST0), gc = LD128(sp8, WS_XG + pj * 16384), gd = LD128(sp8 + 16, WS_XG + pj * 16384);
  BAR();
  STAMP(5);
  expand_kernel(sm, tid, kidx);
  unsigned gf = tag;
  if (w == 7) gf = __builtin_amdgcn_raw_buffer_load_b32(rs, (lane & 31) * 128, WS_WRDY + cp * 4096, SC1);
  STAMP(6);
  v4u W[32];
  {
#define G_OK (ga.y == tag && ga.w == tag && gb.y == tag && gb.w == tag && gc.y == tag && gc.w == tag && gd.y == tag && gd.w == tag)
    if (!wall(G_OK)) {
#pragma unroll 1
      for (int sp = 0;; ++sp) {
        COUNT(60);
        __builtin_amdgcn_s_sleep(2);
        CFENCE();
        ga = LD128(sp8, WS_ST0); gb = LD128(sp8 + 16, WS_ST0); gc = LD128(sp8, WS_XG + pj * 16384); gd = LD128(sp8 + 16, WS_XG + pj * 16384);
        if (wall(G_OK) || sp >= SPIN_CAP) break;
      }
    }
    STAMP(7);
    const unsigned gs[4] = {ga.x, ga.z, gb.x, gb.z};
    float r[4], nb[4];
#pragma unroll
    for (int i = 0; i < 4; ++i) { r[i] = __builtin_amdgcn_rcpf(bfhi(gs[i])); nb[i] = -bflo(gs[i]) * r[i]; }
    stage_conv(sm, sp8 >> 2, v2u{__builtin_amdgcn_perm(gc.z, gc.x, 0x05040100u), __builtin_amdgcn_perm(gd.z, gd.x, 0x05040100u)},
               v2u{__builtin_amdgcn_perm(gc.z, gc.x, 0x07060302u), __builtin_amdgcn_perm(gd.z, gd.x, 0x07060302u)}, r, nb);
    if (w == 7) {
#pragma unroll 1
      for (int sp = 0; !wall(gf == tag) && sp < SPIN_CAP; ++sp) {
        COUNT(59);
        __builtin_amdgcn_s_sleep(2);
        CFENCE();
        gf = __builtin_amdgcn_raw_buffer_load_b32(rs, (lane & 31) * 128, WS_WRDY + cp * 4096, SC1);
      }
      VM0();
    }
    CFENCE();
  }
  layer_body<0>(sm, rs, ws, out, tid, w, bid, rp, cp, pj, r_dsk, r_b1, tag, W, dbg);
  layer_body<1>(sm, rs, ws, out, tid, w, bid, rp, cp, pj, r_dsk, r_b1, tag, W, dbg);
  STAMP(61);
  FLUSH_STAMPS();
}

extern "C" void kernel_launch(void* const* d_in, const int* in_sizes, int n_in, void* d_out, int out_size, void* d_ws, size_t ws_size, hipStream_t stream) {
  (void)in_sizes; (void)n_in; (void)out_size; (void)ws_size;
  (void)hipFuncSetAttribute((const void*)fused_fwd, hipFuncAttributeMaxDynamicSharedMemorySize, LD_END);
  fused_fwd<<<256, 512, LD_END, stream>>>((const u16*)d_in[0], (const u16*)d_in[1], (const u16*)d_in[2], (const u16*)d_in[3], (const u16*)d_in[4],
                                          (const u16*)d_in[5], (const u16*)d_in[6], (const u16*)d_in[8], (const u16*)d_in[9], (const u16*)d_in[10],
                                          (u16*)d_out, (unsigned char*)d_ws);
}
```
